```python
import torch

def _softmax(x, axis=-1):
    return torch.softmax(x, dim=axis)
def _gelu(x):
    return torch.nn.functional.gelu(x, approximate="tanh")
def _silu(x):
    return torch.nn.functional.silu(x)
def _sigmoid(x):
    return torch.sigmoid(x)
def _layernorm(x, axis=-1, eps=1e-5):
    m = x.mean(dim=axis, keepdim=True)
    v = x.var(dim=axis, keepdim=True, unbiased=False)
    return (x - m) / torch.sqrt(v + eps)
def _scatter_add(tgt, idx, src, axis=0):
    return tgt.clone().index_add_(axis, idx, src)
def _segment_sum(src, seg, n):
    out = torch.zeros((n,) + src.shape[1:], dtype=src.dtype)
    return out.index_add_(0, seg, src)
def _pad(x, pw):
    flat = tuple(v for pair in reversed(pw) for v in pair)
    return torch.nn.functional.pad(x, flat)

kernel_name = 'interleave_layers__ssm_conv_s4d__linear_attn_block__prompt'

def setup_inputs():

    _w = (lambda a: (lambda t: t.to(torch.bfloat16) if t.is_floating_point() else t)(torch.as_tensor(a)))
    def _A_setup():
        Bsz, L, D, N = 1, 2048, 512, 128
        return {
            "u": _w((lambda *s: torch.randn(*s, dtype=torch.bfloat16) * 0.05)(Bsz, L, D)),
            "log_A_real": _w((lambda *s: torch.randn(*s, dtype=torch.bfloat16) * 1.6)(D, N) * 0.5),
            "A_imag": _w((lambda *s: torch.randn(*s, dtype=torch.bfloat16) * 0.05)(D, N) * 3.14),
            "C_real": _w((lambda *s: torch.randn(*s, dtype=torch.bfloat16) * 0.05)(D, N) / N ** 0.5),
            "C_imag": _w((lambda *s: torch.randn(*s, dtype=torch.bfloat16) * 0.05)(D, N) / N ** 0.5),
            "log_dt": _w((lambda *s: torch.randn(*s, dtype=torch.bfloat16) * 0.05)(D) * 0.5 - 4.0),
            "D_skip": _w((lambda *s: torch.randn(*s, dtype=torch.bfloat16) * 0.05)(D)),
        }
    _A_in = _A_setup()

    def _B_setup():
        B, S, D = 1, 2048, 512
        return {
            "x": _w((lambda *s: torch.randn(*s, dtype=torch.bfloat16) * 0.05)(B, S, D)),
            "wqkv": _w((lambda *s: torch.randn(*s, dtype=torch.bfloat16) * 0.05)(D, 3 * D)),
            "wo": _w((lambda *s: torch.randn(*s, dtype=torch.bfloat16) * 0.05)(D, D)),
        }
    _B_in = _B_setup()

    return {
        "A_u": _A_in["u"],
        "A_log_A_real": _A_in["log_A_real"],
        "A_A_imag": _A_in["A_imag"],
        "A_C_real": _A_in["C_real"],
        "A_C_imag": _A_in["C_imag"],
        "A_log_dt": _A_in["log_dt"],
        "A_D_skip": _A_in["D_skip"],
        "B_x": _B_in["x"],
        "B_wqkv": _B_in["wqkv"],
        "B_wo": _B_in["wo"],
    }

def reference(A_u, A_log_A_real, A_A_imag, A_C_real, A_C_imag, A_log_dt, A_D_skip, B_x, B_wqkv, B_wo):
    def _A_ref(u, log_A_real, A_imag, C_real, C_imag, log_dt, D_skip):
        L = u.shape[1]
        dt = torch.exp(log_dt)
        A = -torch.exp(log_A_real) + 1j * A_imag
        C = (C_real + 1j * C_imag) * dt[:, None]
        pos = (lambda x, dt: x.to(dt))(torch.arange(L), log_dt.dtype)
        P = torch.exp((A * dt[:, None])[:, :, None] * pos[None, None, :])
        K = 2.0 * torch.einsum("dn,dnl->dl", C, P).real
        L2 = 2 * L
        u_t = (lambda x, *d: x.permute(*d))(u, 0, 2, 1)
        Kf = (lambda x, *a, **k: torch.fft.rfft(x.float(), *a, **k))(K, L2)
        Uf = (lambda x, *a, **k: torch.fft.rfft(x.float(), *a, **k))(u_t, L2)
        y = (lambda x, *a, **k: torch.fft.irfft(x, *a, **k).to(torch.bfloat16))(Uf * Kf[None, :, :], L2)[:, :, :L]
        y = (lambda x, *d: x.permute(*d))(y, 0, 2, 1)
        return y + u * D_skip[None, None, :]

    def _B_ref(x, wqkv, wo):
        B, S, D = x.shape
        H = 32; Dh = D // H
        qkv = (lambda x, *d: x.permute(*d))((x @ wqkv).reshape(B, S, 3, H, Dh), 2, 0, 3, 1, 4)
        q, k, v = qkv[0], qkv[1], qkv[2]
        phi = lambda t: torch.where(t > 0, t + 1, torch.exp(t)) + 1e-6
        pq, pk = phi(q), phi(k)
        kv = torch.einsum("bhsd,bhse->bhde", pk, v)
        z = torch.einsum("bhsd->bhd", pk)
        num = torch.einsum("bhqd,bhde->bhqe", pq, kv)
        den = torch.einsum("bhqd,bhd->bhq", pq, z)[..., None] + 1e-6
        o = num / den
        return (lambda x, *d: x.permute(*d))(o, 0, 2, 1, 3).reshape(B, S, D) @ wo

    x = A_u
    x = x + _A_ref(_layernorm(x, axis=-1), A_log_A_real, A_A_imag, A_C_real, A_C_imag, A_log_dt, A_D_skip)
    x = x + _B_ref(_layernorm(x, axis=-1), B_wqkv, B_wo)
    x = x + _A_ref(_layernorm(x, axis=-1), A_log_A_real, A_A_imag, A_C_real, A_C_imag, A_log_dt, A_D_skip)
    x = x + _B_ref(_layernorm(x, axis=-1), B_wqkv, B_wo)
    return x
```

```cpp
#include <hip/hip_runtime.h>
#include <hip/hip_bf16.h>
#include <stdint.h>

typedef unsigned short u16;
typedef unsigned int u32;
typedef __attribute__((ext_vector_type(4))) float f32x4;
typedef __attribute__((ext_vector_type(2))) float f32x2;
typedef __attribute__((ext_vector_type(8))) __bf16 bf16x8;
typedef __attribute__((ext_vector_type(2))) __bf16 bf16x2;
typedef __attribute__((ext_vector_type(8))) _Float16 f16x8;
typedef __attribute__((ext_vector_type(2))) _Float16 f16x2;
typedef __attribute__((ext_vector_type(4))) short s16x4;
typedef __attribute__((ext_vector_type(8))) short s16x8;
typedef __attribute__((ext_vector_type(4))) u32 u32x4;
typedef __attribute__((ext_vector_type(2))) u32 u32x2;
typedef __attribute__((address_space(3))) s16x4* lds_s16x4_p;

#define DEVI __device__ __forceinline__

constexpr int SL = 2048;
constexpr int DM = 512;
constexpr int NST = 128;
constexpr int KRL = 2056;
constexpr int KVH = 272;

DEVI float bf_lo(u32 v) { return __uint_as_float(v << 16); }
DEVI float bf_hi(u32 v) { return __uint_as_float(v & 0xffff0000u); }
DEVI float bf2f(u16 v) { return __uint_as_float(((u32)v) << 16); }
DEVI u32 pk_bf(float a, float b) {
    f32x2 x = {a, b};
    return __builtin_bit_cast(u32, __builtin_convertvector(x, bf16x2));
}
DEVI u32 pk_h(float a, float b) {
    f32x2 x = {a, b};
    return __builtin_bit_cast(u32, __builtin_convertvector(x, f16x2));
}
DEVI float rbf(float a) { return bf_lo(pk_bf(a, 0.f)); }
template <int C> DEVI float dpp_f(float x) {
    return __int_as_float(__builtin_amdgcn_update_dpp(0, __float_as_int(x), C, 0xf, 0xf, true));
}
DEVI float row16_sum(float x) {
    x += dpp_f<0xB1>(x); x += dpp_f<0x4E>(x); x += dpp_f<0x141>(x); x += dpp_f<0x140>(x);
    return x;
}
DEVI float rdl(float x, int l) { return __int_as_float(__builtin_amdgcn_readlane(__float_as_int(x), l)); }
DEVI float wave_sum(float x) {
    x = row16_sum(x);
    return (rdl(x, 0) + rdl(x, 16)) + (rdl(x, 32) + rdl(x, 48));
}
DEVI float wave_max(float x) {
    x = fmaxf(x, dpp_f<0xB1>(x)); x = fmaxf(x, dpp_f<0x4E>(x));
    x = fmaxf(x, dpp_f<0x141>(x)); x = fmaxf(x, dpp_f<0x140>(x));
    return fmaxf(fmaxf(rdl(x, 0), rdl(x, 16)), fmaxf(rdl(x, 32), rdl(x, 48)));
}
DEVI f32x4 mfma_bf(bf16x8 a, bf16x8 b, f32x4 c) { return __builtin_amdgcn_mfma_f32_16x16x32_bf16(a, b, c, 0, 0, 0); }
DEVI f32x4 mfma_h(f16x8 a, f16x8 b, f32x4 c) { return __builtin_amdgcn_mfma_f32_16x16x32_f16(a, b, c, 0, 0, 0); }
DEVI f32x4 mfma_bf16k(s16x4 a, s16x4 b, f32x4 c) { return __builtin_amdgcn_mfma_f32_16x16x16bf16_1k(a, b, c, 0, 0, 0); }
DEVI s16x4 lds_tr(const char* p) { return __builtin_amdgcn_ds_read_tr16_b64_v4i16((lds_s16x4_p)p); }
DEVI void ln_scalars(float mean, float var, float& mb, float& rs) {
    mb = rbf(mean);
    float sb = rbf(sqrtf(rbf(rbf(var) + 1e-5f)));
    rs = 1.0f / sb;
}

constexpr int P_TR = 64, P_WQ = 96, P_WO = 32, P_KG = 512;
constexpr int TRP = 1088;

__global__ __launch_bounds__(256, 2) void prep_kernel(
    const u16* __restrict__ x, const u16* __restrict__ lar, const u16* __restrict__ aim,
    const u16* __restrict__ cre, const u16* __restrict__ cim, const u16* __restrict__ ldt,
    const u16* __restrict__ wqkv, const u16* __restrict__ wo,
    u16* __restrict__ xT, f32x2* __restrict__ stats, u16* __restrict__ Kr, float* __restrict__ Kscale,
    u16* __restrict__ wqF, float* __restrict__ Wsum, u16* __restrict__ woF, float* __restrict__ kvacc) {
    __shared__ __attribute__((aligned(16))) char sm[32 * TRP];
    const int t = threadIdx.x, lane = t & 63, w = t >> 6, g = lane >> 4, j = lane & 15;
    int b = blockIdx.x;
    if (b < P_TR) {
        u32x4 v[8];
#pragma unroll
        for (int i = 0; i < 8; ++i) v[i] = *(const u32x4*)(x + (size_t)(32 * b + 8 * w + i) * DM + lane * 8);
#pragma unroll
        for (int i = 0; i < 8; ++i) {
            *(u32x4*)(sm + (8 * w + i) * TRP + lane * 16) = v[i];
            float f[8];
#pragma unroll
            for (int e = 0; e < 4; ++e) { f[2 * e] = bf_lo(v[i][e]); f[2 * e + 1] = bf_hi(v[i][e]); }
            float s = ((f[0] + f[1]) + (f[2] + f[3])) + ((f[4] + f[5]) + (f[6] + f[7]));
            float mean = wave_sum(s) * (1.0f / DM);
            float q = 0.f;
#pragma unroll
            for (int e = 0; e < 8; ++e) { float d = f[e] - mean; q = fmaf(d, d, q); }
            float m2 = wave_sum(q);
            if (lane == 0) stats[32 * b + 8 * w + i] = f32x2{mean, m2};
        }
        __syncthreads();
#pragma unroll
        for (int cb = 0; cb < 2; ++cb) {
            int c0 = w * 128 + cb * 64 + 16 * g;
            const char* base = sm + ((lane >> 2) & 3) * TRP + (c0 + 4 * (lane & 3)) * 2;
            s16x4 r[8];
#pragma unroll
            for (int i = 0; i < 8; ++i) r[i] = lds_tr(base + 4 * i * TRP);
            u16* dst = xT + (size_t)(c0 + j) * SL + 32 * b;
#pragma unroll
            for (int i = 0; i < 4; ++i) {
                u32x2 a = __builtin_bit_cast(u32x2, r[2 * i]), c = __builtin_bit_cast(u32x2, r[2 * i + 1]);
                *(u32x4*)(dst + 8 * i) = u32x4{a[0], a[1], c[0], c[1]};
            }
        }
        return;
    }
    b -= P_TR;
    if (b < P_WQ) {
        const int hp = b / 6, nt = b % 6;
        const int col = (nt % 3) * DM + (2 * hp + nt / 3) * 16 + j;
        float s = 0.f;
#pragma unroll
        for (int i = 0; i < 4; ++i) {
            int ks = 4 * w + i;
            u16 e[8];
#pragma unroll
            for (int jj = 0; jj < 8; ++jj) e[jj] = wqkv[(size_t)(32 * ks + 8 * g + jj) * (3 * DM) + col];
            u32x4 o;
#pragma unroll
            for (int jj = 0; jj < 4; ++jj) {
                o[jj] = (u32)e[2 * jj] | ((u32)e[2 * jj + 1] << 16);
                s += bf2f(e[2 * jj]) + bf2f(e[2 * jj + 1]);
            }
            *(u32x4*)(wqF + ((size_t)((hp * 16 + ks) * 6 + nt) * 64 + lane) * 8) = o;
        }
        float* red = (float*)sm;
        red[t] = s;
        __syncthreads();
        if (t < 16) {
            float a = 0.f;
#pragma unroll
            for (int i = 0; i < 16; ++i) a += red[i * 16 + t];
            Wsum[col] = a;
        }
        return;
    }
    b -= P_WQ;
    if (b < P_WO) {
#pragma unroll
        for (int i = 0; i < 4; ++i) {
            int hp = 4 * w + i;
            u16 e[8];
#pragma unroll
            for (int jj = 0; jj < 8; ++jj)
                e[jj] = wo[(size_t)((2 * hp + (jj >> 2)) * 16 + 4 * g + (jj & 3)) * DM + b * 16 + j];
            u32x4 o;
#pragma unroll
            for (int jj = 0; jj < 4; ++jj) o[jj] = (u32)e[2 * jj] | ((u32)e[2 * jj + 1] << 16);
            *(u32x4*)(woF + ((size_t)(b * 16 + hp) * 64 + lane) * 8) = o;
        }
        for (int i = t; i < 2 * KVH; i += 256) kvacc[b * 2 * KVH + i] = 0.f;
        return;
    }
    b -= P_WO;
    {
        const int d = b;
        const u16* pc = cre + d * NST; const u16* pi = cim + d * NST;
        float cm = fmaxf(fabsf(bf2f(pc[lane])) + fabsf(bf2f(pi[lane])),
                         fabsf(bf2f(pc[lane + 64])) + fabsf(bf2f(pi[lane + 64])));
        u32x2 vl[2], va[2], vc[2], vi[2];
#pragma unroll
        for (int ts = 0; ts < 2; ++ts) {
            int n0 = d * NST + 32 * w + 16 * ts + 4 * g;
            vl[ts] = *(const u32x2*)(lar + n0); va[ts] = *(const u32x2*)(aim + n0);
            vc[ts] = *(const u32x2*)(cre + n0); vi[ts] = *(const u32x2*)(cim + n0);
        }
        const float dt = rbf(expf(bf2f(ldt[d])));
        cm = wave_max(cm);
        const float cinv = cm > 0.f ? 1.0f / cm : 0.f;
        f32x4 acc[4];
#pragma unroll
        for (int s = 0; s < 4; ++s) acc[s] = f32x4{0.f, 0.f, 0.f, 0.f};
        const float fj = (float)j;
#pragma unroll
        for (int ts = 0; ts < 2; ++ts) {
            u32x4 fr[8];
#pragma unroll
            for (int e = 0; e < 4; ++e) {
                u32 wl = vl[ts][e >> 1], wa = va[ts][e >> 1], wc = vc[ts][e >> 1], wi = vi[ts][e >> 1];
                float fl = (e & 1) ? bf_hi(wl) : bf_lo(wl), fa = (e & 1) ? bf_hi(wa) : bf_lo(wa);
                float fc = ((e & 1) ? bf_hi(wc) : bf_lo(wc)) * cinv, fi = ((e & 1) ? bf_hi(wi) : bf_lo(wi)) * cinv;
                float are = -rbf(expf(fl)) * dt, aimdt = fa * dt;
                float mag = __builtin_amdgcn_exp2f(are * 1.4426950408889634f * fj);
                float rev = aimdt * 0.15915494309189535f * fj;
                rev -= rintf(rev);
                float pr = mag * __builtin_amdgcn_cosf(rev), pi_ = mag * __builtin_amdgcn_sinf(rev);
#pragma unroll
                for (int k = 0; k < 8; ++k) {
                    if (k > 0) {
                        float nr = fmaf(pr, pr, -pi_ * pi_), ni = 2.0f * pr * pi_;
                        pr = nr; pi_ = ni;
                    }
                    float qr = pr, qi = pi_;
                    if (k == 7) {
                        float sr = __shfl(pr, (lane & 48) | 8), si = __shfl(pi_, (lane & 48) | 8);
                        if (j == 0) { qr = fmaf(sr, sr, -si * si); qi = 2.0f * sr * si; }
                    }
                    if (k < 4) fr[k][e] = pk_h(qr, qi);
                    else fr[k][e] = pk_h(fmaf(fc, qr, -fi * qi), -fmaf(fc, qi, fi * qr));
                }
            }
#pragma unroll
            for (int s = 0; s < 4; ++s)
                acc[s] = mfma_h(__builtin_bit_cast(f16x8, fr[s]), __builtin_bit_cast(f16x8, fr[4 + s]), acc[s]);
        }
        float* part = (float*)sm;
        float* tab = part + 4096;
#pragma unroll
        for (int s = 0; s < 4; ++s) *(f32x4*)(part + (w * 4 + s) * 256 + j * 16 + 4 * g) = acc[s];
        __syncthreads();
#pragma unroll
        for (int s = 0; s < 4; ++s)
            tab[s * 256 + t] = (part[s * 256 + t] + part[(4 + s) * 256 + t]) + (part[(8 + s) * 256 + t] + part[(12 + s) * 256 + t]);
        __syncthreads();
        float kv[8];
#pragma unroll
        for (int e = 0; e < 8; ++e) {
            int l = 2047 - (8 * t + e);
            int sh = l < 256 ? 0 : (l < 512 ? 1 : (l < 1024 ? 2 : 3));
            int q = sh ? ((l + (1 << (sh - 1)) - 1 + ((l >> sh) & 1)) >> sh) : l;
            if (q == 256) { if (sh < 3) { ++sh; q = 128; } else q = 0; }
            kv[e] = tab[sh * 256 + q];
        }
        u16* dst = Kr + (size_t)d * KRL;
        *(u32x4*)(dst + 8 * t) = u32x4{pk_h(kv[0], kv[1]), pk_h(kv[2], kv[3]), pk_h(kv[4], kv[5]), pk_h(kv[6], kv[7])};
        if (t == 0) { *(u32x4*)(dst + 2048) = u32x4{0, 0, 0, 0}; Kscale[d] = 2.0f * dt * cm; }
    }
}

constexpr int RCP = 4160;
constexpr int UP = (256 + SL) * 2;
template <int NCT>
__global__ __launch_bounds__(256, 1) void conv_kernel(
    const u16* __restrict__ xT, const f32x2* __restrict__ stats, const u16* __restrict__ Kr,
    const float* __restrict__ Kscale, const u16* __restrict__ dskip, u16* __restrict__ xo) {
    __shared__ __attribute__((aligned(16))) char sm[8 * RCP + 2 * UP + 16384 + 16384];
    char* RC = sm; char* U = sm + 8 * RCP; char* XU = U + 2 * UP; char* RED = XU + 16384;
    const int t = threadIdx.x, lane = t & 63, w = t >> 6, g = lane >> 4, r = lane & 15;
    const int c0 = 2 * blockIdx.x;
    u32x4 xv[2], ka[2]; u32x2 kb[2];
#pragma unroll
    for (int ch = 0; ch < 2; ++ch) {
        xv[ch] = *(const u32x4*)(xT + (size_t)(c0 + ch) * SL + 8 * t);
        ka[ch] = *(const u32x4*)(Kr + (size_t)(c0 + ch) * KRL + 8 * t);
        kb[ch] = *(const u32x2*)(Kr + (size_t)(c0 + ch) * KRL + 8 * t + 8);
    }
    f32x4 sv[NCT][4];
#pragma unroll
    for (int ct = 0; ct < NCT; ++ct)
#pragma unroll
        for (int i = 0; i < 4; ++i) sv[ct][i] = *(const f32x4*)(stats + ct * SL + 8 * t + 2 * i);
    const float ds[2] = {bf2f(dskip[c0]), bf2f(dskip[c0 + 1])};
    const float ksc = Kscale[c0 + (w >> 1)];
    if (t < 64) *(u32x4*)(U + (t >> 5) * UP + (t & 31) * 16) = u32x4{0, 0, 0, 0};
    else if (t < 80) *(u32x4*)(RC + ((t - 64) >> 1) * RCP + 4096 + (t & 1) * 16) = u32x4{0, 0, 0, 0};
#pragma unroll
    for (int ch = 0; ch < 2; ++ch) {
        u32 d0 = ka[ch][0], d1 = ka[ch][1], d2 = ka[ch][2], d3 = ka[ch][3], d4 = kb[ch][0], d5 = kb[ch][1];
        u32 a0 = __builtin_amdgcn_alignbit(d1, d0, 16), a1 = __builtin_amdgcn_alignbit(d2, d1, 16);
        u32 a2 = __builtin_amdgcn_alignbit(d3, d2, 16), a3 = __builtin_amdgcn_alignbit(d4, d3, 16);
        u32 a4 = __builtin_amdgcn_alignbit(d5, d4, 16);
        char* p = RC + ch * 4 * RCP + t * 16;
        *(u32x4*)(p) = u32x4{d0, d1, d2, d3};
        *(u32x4*)(p + RCP) = u32x4{a0, a1, a2, a3};
        *(u32x4*)(p + 2 * RCP) = u32x4{d1, d2, d3, d4};
        *(u32x4*)(p + 3 * RCP) = u32x4{a1, a2, a3, a4};
    }
    float mb[8], rs[8];
#pragma unroll
    for (int e = 0; e < 8; ++e) {
        float mean, m2;
        if (NCT == 1) { mean = sv[0][e >> 1][(e & 1) * 2]; m2 = sv[0][e >> 1][(e & 1) * 2 + 1]; }
        else {
            mean = 0.f; m2 = 0.f;
#pragma unroll
            for (int ct = 0; ct < NCT; ++ct) { mean += sv[ct][e >> 1][(e & 1) * 2]; m2 += sv[ct][e >> 1][(e & 1) * 2 + 1]; }
            mean *= (1.0f / NCT);
#pragma unroll
            for (int ct = 0; ct < NCT; ++ct) { float dd = sv[ct][e >> 1][(e & 1) * 2] - mean; m2 = fmaf(dd * (float)(DM / NCT), dd, m2); }
        }
        ln_scalars(mean, m2 * (1.0f / DM), mb[e], rs[e]);
    }
#pragma unroll
    for (int ch = 0; ch < 2; ++ch) {
        float uu[8]; u32 xu[8];
#pragma unroll
        for (int e = 0; e < 8; ++e) {
            u32 wd = xv[ch][e >> 1];
            float xf = (e & 1) ? bf_hi(wd) : bf_lo(wd);
            uu[e] = rbf(rbf(xf - mb[e]) * rs[e]);
            xu[e] = pk_bf(xf, uu[e] * ds[ch]);
        }
        *(u32x4*)(U + ch * UP + 512 + t * 16) = u32x4{pk_h(uu[0], uu[1]), pk_h(uu[2], uu[3]), pk_h(uu[4], uu[5]), pk_h(uu[6], uu[7])};
        *(u32x4*)(XU + ch * 8192 + t * 32) = u32x4{xu[0], xu[1], xu[2], xu[3]};
        *(u32x4*)(XU + ch * 8192 + t * 32 + 16) = u32x4{xu[4], xu[5], xu[6], xu[7]};
    }
    __syncthreads();
    const int ch = w >> 1, mh = w & 1;
    f32x4 acc[8];
#pragma unroll
    for (int i = 0; i < 8; ++i) acc[i] = f32x4{0.f, 0.f, 0.f, 0.f};
    const char* fb = RC + (ch * 4 + 3 - (r & 3)) * RCP + 2 * (2028 - 32 * (4 * mh) - 256 * 7 - (r & ~3) + 8 * g) - 192;
    const char* ub = U + ch * UP + 2 * (256 - 32 * (4 * mh) - 16 + 16 * r + 8 * g) - 192;
#pragma unroll
    for (int mu = 0; mu < 4; ++mu) {
        f16x8 F[8], B[8];
#pragma unroll
        for (int i = 0; i < 8; ++i) {
            const char* p = fb + 192 - 64 * mu + 512 * (7 - i);
            u32x2 lo = *(const u32x2*)p, hi = *(const u32x2*)(p + 8);
            F[i] = __builtin_bit_cast(f16x8, u32x4{lo[0], lo[1], hi[0], hi[1]});
            B[i] = *(const f16x8*)(ub + 192 - 64 * mu + 512 * i);
        }
#pragma unroll
        for (int i = 0; i < 8; ++i)
#pragma unroll
            for (int tt = i; tt < 8; ++tt) acc[tt] = mfma_h(F[i], B[tt - i], acc[tt]);
    }
#pragma unroll
    for (int i = 0; i < 4; ++i) {
        f32x4 lo = acc[i], hi = acc[4 + i];
        *(f32x4*)(RED + ((ch * 2 + mh) * 4 + i) * 1024 + lane * 16) = mh ? lo : hi;
    }
    __syncthreads();
    u16* dst = xo + (size_t)(c0 + ch) * SL;
#pragma unroll
    for (int i = 0; i < 4; ++i) {
        f32x4 o = *(const f32x4*)(RED + ((ch * 2 + (mh ^ 1)) * 4 + i) * 1024 + lane * 16);
        f32x4 lo = acc[i], hi = acc[4 + i];
        f32x4 y = (mh ? hi : lo) + o;
        int l = 256 * (4 * mh + i) + 16 * r + 4 * g;
        u32x4 xu = *(const u32x4*)(XU + ch * 8192 + l * 4);
        float res[4];
#pragma unroll
        for (int e = 0; e < 4; ++e) res[e] = bf_lo(xu[e]) + rbf(rbf(y[e] * ksc) + bf_hi(xu[e]));
        *(u32x2*)(dst + l) = u32x2{pk_bf(res[0], res[1]), pk_bf(res[2], res[3])};
    }
}

constexpr int QSLOT = 28672, QNS = 5;
DEVI float phi_map(float tq) {
    float p = tq > 0.f ? rbf(tq + 1.0f) : rbf(__expf(tq));
    return rbf(p + 1e-6f);
}
template <int N> DEVI void wait_vm() { asm volatile("s_waitcnt vmcnt(%0)" ::"n"(N) : "memory"); }

__global__ __launch_bounds__(256, 1) void qkv_kernel(
    const u16* __restrict__ xT, const u16* __restrict__ wqF, const float* __restrict__ Wsum,
    u16* __restrict__ pqF, float* __restrict__ kvacc) {
    __shared__ __attribute__((aligned(16))) char ring[QNS * QSLOT + 8192];
    char* sm = ring + QNS * QSLOT;
    const int t = threadIdx.x, lane = t & 63, g = lane >> 4, j = lane & 15;
    const int w = __builtin_amdgcn_readfirstlane(t >> 6), rg = w >> 1, hh = w & 1;
    const int xcd = blockIdx.x & 7, jb = blockIdx.x >> 3;
    const int rt = 4 * (xcd >> 1) + (jb & 3), hp = 8 * (xcd & 1) + (jb >> 2);
    const int head = 2 * hp + hh;
    const float wsq0 = Wsum[head * 16 + 4 * g], wsq1 = Wsum[head * 16 + 4 * g + 1];
    const float wsq2 = Wsum[head * 16 + 4 * g + 2], wsq3 = Wsum[head * 16 + 4 * g + 3];
    const float wsk = Wsum[DM + head * 16 + j], wsv = Wsum[2 * DM + head * 16 + j];
    const u16* xs[4];
#pragma unroll
    for (int i = 0; i < 4; ++i) {
        int row = 4 * (4 * w + i) + g, c16 = j;
        int f = (row & 3) | (((row >> 3) & 1) << 2);
        xs[i] = xT + (size_t)row * SL + rt * 128 + ((c16 >> 1) ^ f) * 16 + (c16 & 1) * 8;
    }
    const u16* wsrc = wqF + (size_t)hp * (16 * 6 * 512) + (3 * w) * 512 + lane * 8;
    auto issue = [&](int kc) {
        char* slot = ring + (kc % QNS) * QSLOT;
#pragma unroll
        for (int i = 0; i < 4; ++i)
            __builtin_amdgcn_global_load_lds((const u32*)(xs[i] + (size_t)kc * 64 * SL), (u32*)(slot + (4 * w + i) * 1024), 16, 0, 0);
#pragma unroll
        for (int i = 0; i < 3; ++i)
            __builtin_amdgcn_global_load_lds((const u32*)(wsrc + kc * (12 * 512) + i * 512), (u32*)(slot + 16384 + (3 * w + i) * 1024), 16, 0, 0);
    };
#pragma unroll
    for (int kc = 0; kc < QNS; ++kc) issue(kc);

    f32x4 aq[4], ak[4], av[4], as[4];
#pragma unroll
    for (int i = 0; i < 4; ++i) { aq[i] = ak[i] = av[i] = as[i] = f32x4{0.f, 0.f, 0.f, 0.f}; }
    const u32 ones = 0x3f803f80u;
    const bf16x8 onesv = __builtin_bit_cast(bf16x8, u32x4{ones, ones, ones, ones});
    const int q4 = (lane >> 2) & 3, fl = q4 | ((g & 1) << 2);
    const u32 lbase = (u32)(uintptr_t)(__attribute__((address_space(3))) char*)ring;
    u32 xoff[4];
#pragma unroll
    for (int mt = 0; mt < 4; ++mt) xoff[mt] = lbase + (8 * g + q4) * 256 + (((4 * rg + mt) ^ fl) * 32) + (lane & 3) * 8;
    const u32 woff = lbase + 16384 + hh * 3 * 1024 + lane * 16;
    struct Frag { u32x2 lo[4], hi[4]; u32x4 wq, wk, wv; };
    auto rd = [&](Frag& f, int kc, int ks) {
        const u32 so = (kc % QNS) * QSLOT;
#pragma unroll
        for (int mt = 0; mt < 4; ++mt) {
            u32 a = xoff[mt] + so;
            if (ks == 0) {
                asm volatile("ds_read_b64_tr_b16 %0, %1" : "=v"(f.lo[mt]) : "v"(a));
                asm volatile("ds_read_b64_tr_b16 %0, %1 offset:1024" : "=v"(f.hi[mt]) : "v"(a));
            } else {
                asm volatile("ds_read_b64_tr_b16 %0, %1 offset:8192" : "=v"(f.lo[mt]) : "v"(a));
                asm volatile("ds_read_b64_tr_b16 %0, %1 offset:9216" : "=v"(f.hi[mt]) : "v"(a));
            }
        }
        u32 a = woff + so;
        if (ks == 0) {
            asm volatile("ds_read_b128 %0, %1" : "=v"(f.wq) : "v"(a));
            asm volatile("ds_read_b128 %0, %1 offset:1024" : "=v"(f.wk) : "v"(a));
            asm volatile("ds_read_b128 %0, %1 offset:2048" : "=v"(f.wv) : "v"(a));
        } else {
            asm volatile("ds_read_b128 %0, %1 offset:6144" : "=v"(f.wq) : "v"(a));
            asm volatile("ds_read_b128 %0, %1 offset:7168" : "=v"(f.wk) : "v"(a));
            asm volatile("ds_read_b128 %0, %1 offset:8192" : "=v"(f.wv) : "v"(a));
        }
    };
#define Q_WAIT(F, N) asm volatile("s_waitcnt lgkmcnt(" #N ")" : "+v"(F.lo[0]), "+v"(F.hi[0]), "+v"(F.lo[1]), "+v"(F.hi[1]), \
        "+v"(F.lo[2]), "+v"(F.hi[2]), "+v"(F.lo[3]), "+v"(F.hi[3]), "+v"(F.wq), "+v"(F.wk), "+v"(F.wv) :: "memory")
    auto mm = [&](const Frag& f) {
        bf16x8 wq = __builtin_bit_cast(bf16x8, f.wq), wk = __builtin_bit_cast(bf16x8, f.wk), wv = __builtin_bit_cast(bf16x8, f.wv);
        bf16x8 xa[4];
#pragma unroll
        for (int mt = 0; mt < 4; ++mt) xa[mt] = __builtin_bit_cast(bf16x8, u32x4{f.lo[mt][0], f.lo[mt][1], f.hi[mt][0], f.hi[mt][1]});
#pragma unroll
        for (int mt = 0; mt < 4; ++mt) {
            aq[mt] = mfma_bf(wq, xa[mt], aq[mt]);
            ak[mt] = mfma_bf(xa[mt], wk, ak[mt]);
            av[mt] = mfma_bf(xa[mt], wv, av[mt]);
        }
        if (hh) {
#pragma unroll
            for (int mt = 0; mt < 4; ++mt) as[mt] = mfma_bf(xa[mt], onesv, as[mt]);
        } else {
#pragma unroll
            for (int mt = 0; mt < 4; ++mt) as[mt] = mfma_bf(xa[mt], xa[mt], as[mt]);
        }
    };
    Frag fa, fb;
    wait_vm<28>();
    __builtin_amdgcn_s_barrier();
    rd(fa, 0, 0);
#pragma unroll
    for (int kc = 0; kc < 8; ++kc) {
        rd(fb, kc, 1);
        Q_WAIT(fa, 11);
        mm(fa);
        Q_WAIT(fb, 0);
        if (kc < 7) {
            if (kc <= 3) wait_vm<21>(); else if (kc == 4) wait_vm<14>(); else if (kc == 5) wait_vm<7>(); else wait_vm<0>();
            __builtin_amdgcn_s_barrier();
            asm volatile("" ::: "memory");
            if (kc + QNS < 8) issue(kc + QNS);
            rd(fa, kc + 1, 0);
        }
        mm(fb);
    }
#undef Q_WAIT
    float* st = (float*)sm;
    f32x2* ln = (f32x2*)(st + 256);
    float* red = (float*)(ln + 128);
#pragma unroll
    for (int mt = 0; mt < 4; ++mt) {
        int rgi = j & 3;
        float v = rgi == 0 ? as[mt][0] : (rgi == 1 ? as[mt][1] : (rgi == 2 ? as[mt][2] : as[mt][3]));
        if (g == (j >> 2)) st[(hh ^ 1) * 128 + rg * 64 + mt * 16 + j] = v;
    }
    __syncthreads();
    if (t < 128) {
        float sum = st[t], sq = st[128 + t];
        float mean = sum * (1.0f / DM);
        float var = fmaxf(fmaf(-mean, mean, sq * (1.0f / DM)), 0.f);
        float mb, rs;
        ln_scalars(mean, var, mb, rs);
        ln[t] = f32x2{mb, rs};
    }
    __syncthreads();
    const float wsq[4] = {wsq0, wsq1, wsq2, wsq3};
    u32 pk8[2][4], vv8[2][4];
#pragma unroll
    for (int mt = 0; mt < 4; ++mt) {
        f32x2 s1 = ln[rg * 64 + mt * 16 + j];
        float p[4];
#pragma unroll
        for (int e = 0; e < 4; ++e) p[e] = phi_map(rbf((aq[mt][e] - s1[0] * wsq[e]) * s1[1]));
        int lt = rt * 8 + rg * 4 + mt;
        *(u32x2*)(pqF + ((size_t)(lt * 16 + hp) * 64 + lane) * 8 + hh * 4) = u32x2{pk_bf(p[0], p[1]), pk_bf(p[2], p[3])};
        f32x4 sa = *(const f32x4*)(ln + rg * 64 + mt * 16 + 4 * g), sb = *(const f32x4*)(ln + rg * 64 + mt * 16 + 4 * g + 2);
        float mr[4] = {sa[0], sa[2], sb[0], sb[2]}, rr[4] = {sa[1], sa[3], sb[1], sb[3]};
        float kk[4], vv[4];
#pragma unroll
        for (int e = 0; e < 4; ++e) {
            kk[e] = phi_map(rbf((ak[mt][e] - mr[e] * wsk) * rr[e]));
            vv[e] = (av[mt][e] - mr[e] * wsv) * rr[e];
        }
        pk8[mt >> 1][(mt & 1) * 2] = pk_bf(kk[0], kk[1]); pk8[mt >> 1][(mt & 1) * 2 + 1] = pk_bf(kk[2], kk[3]);
        vv8[mt >> 1][(mt & 1) * 2] = pk_bf(vv[0], vv[1]); vv8[mt >> 1][(mt & 1) * 2 + 1] = pk_bf(vv[2], vv[3]);
    }
    f32x4 kvT = {0.f, 0.f, 0.f, 0.f}, zz = {0.f, 0.f, 0.f, 0.f};
#pragma unroll
    for (int i = 0; i < 2; ++i) {
        bf16x8 pk = __builtin_bit_cast(bf16x8, u32x4{pk8[i][0], pk8[i][1], pk8[i][2], pk8[i][3]});
        bf16x8 vv = __builtin_bit_cast(bf16x8, u32x4{vv8[i][0], vv8[i][1], vv8[i][2], vv8[i][3]});
        kvT = mfma_bf(vv, pk, kvT);
        zz = mfma_bf(onesv, pk, zz);
    }
    float* rp = red + (hh * 2 + rg) * KVH;
#pragma unroll
    for (int e = 0; e < 4; ++e) rp[(4 * g + e) * 16 + j] = kvT[e];
    if (g == 0) rp[256 + j] = zz[0];
    __syncthreads();
    for (int i = t; i < 2 * KVH; i += 256) {
        int h2 = i >= KVH, ii = i - h2 * KVH;
        float v = red[(h2 * 2) * KVH + ii] + red[(h2 * 2 + 1) * KVH + ii];
        unsafeAtomicAdd(kvacc + (2 * hp + h2) * KVH + ii, v);
    }
}

template <int FINAL>
__global__ __launch_bounds__(512, 1) void oproj_kernel(
    const u16* __restrict__ xT, const u16* __restrict__ pqF, const float* __restrict__ kvacc,
    const u16* __restrict__ woF, u16* __restrict__ xo, f32x2* __restrict__ stats) {
    __shared__ __attribute__((aligned(16))) char sm[32768 + 128 * 72 + 32 * 272 + 2048];
    char* OF = sm; char* XT = sm + 32768; char* OT = XT + 128 * 72; f32x2* ST = (f32x2*)(OT + 32 * 272);
    const int t = threadIdx.x, lane = t & 63, w = t >> 6, g = lane >> 4, j = lane & 15;
    const int xcd = blockIdx.x & 7, jb = blockIdx.x >> 3;
    const int rt = xcd * 8 + (jb >> 2), cq = jb & 3, l0 = rt * 32;
    u32x4 pq[2][2];
#pragma unroll
    for (int lt = 0; lt < 2; ++lt)
#pragma unroll
        for (int i = 0; i < 2; ++i) pq[lt][i] = *(const u32x4*)(pqF + ((size_t)((2 * rt + lt) * 16 + 2 * w + i) * 64 + lane) * 8);
    f32x4 kf[4], zf[4];
#pragma unroll
    for (int i = 0; i < 4; ++i) {
        kf[i] = *(const f32x4*)(kvacc + (4 * w + i) * KVH + j * 16 + 4 * g);
        zf[i] = *(const f32x4*)(kvacc + (4 * w + i) * KVH + 256 + 4 * g);
    }
    u32x4 xin = *(const u32x4*)(xT + (size_t)(cq * 128 + (t >> 2)) * SL + l0 + (t & 3) * 8);
    bf16x8 wf[16];
#pragma unroll
    for (int ks = 0; ks < 16; ++ks) wf[ks] = *(const bf16x8*)(woF + ((size_t)((cq * 8 + w) * 16 + ks) * 64 + lane) * 8);
    {
        char* p = XT + (t >> 2) * 72 + (t & 3) * 16;
        *(u32x2*)p = u32x2{xin[0], xin[1]}; *(u32x2*)(p + 8) = u32x2{xin[2], xin[3]};
    }
#pragma unroll
    for (int i = 0; i < 2; ++i) {
        u32x4 of[2];
#pragma unroll
        for (int h2 = 0; h2 < 2; ++h2) {
            f32x4 k4 = kf[2 * i + h2], z4 = zf[2 * i + h2];
            s16x4 ka = __builtin_bit_cast(s16x4, u32x2{pk_bf(k4[0], k4[1]), pk_bf(k4[2], k4[3])});
            s16x4 za = __builtin_bit_cast(s16x4, u32x2{pk_bf(z4[0], z4[1]), pk_bf(z4[2], z4[3])});
#pragma unroll
            for (int lt = 0; lt < 2; ++lt) {
                s16x4 b = __builtin_bit_cast(s16x4, u32x2{pq[lt][i][2 * h2], pq[lt][i][2 * h2 + 1]});
                f32x4 zero = {0.f, 0.f, 0.f, 0.f};
                f32x4 num = mfma_bf16k(ka, b, zero), den = mfma_bf16k(za, b, zero);
                float rd = 1.0f / rbf(rbf(den[0]) + 1e-6f);
                of[lt][2 * h2] = pk_bf(rbf(num[0]) * rd, rbf(num[1]) * rd);
                of[lt][2 * h2 + 1] = pk_bf(rbf(num[2]) * rd, rbf(num[3]) * rd);
            }
        }
#pragma unroll
        for (int lt = 0; lt < 2; ++lt) *(u32x4*)(OF + (lt * 16 + 2 * w + i) * 1024 + lane * 16) = of[lt];
    }
    __syncthreads();
    f32x4 acc[2] = {{0.f, 0.f, 0.f, 0.f}, {0.f, 0.f, 0.f, 0.f}};
#pragma unroll
    for (int ks = 0; ks < 16; ++ks)
#pragma unroll
        for (int lt = 0; lt < 2; ++lt)
            acc[lt] = mfma_bf(wf[ks], *(const bf16x8*)(OF + (lt * 16 + ks) * 1024 + lane * 16), acc[lt]);
#pragma unroll
    for (int lt = 0; lt < 2; ++lt) {
        float r[4];
#pragma unroll
        for (int e = 0; e < 4; ++e) {
            u16* px = (u16*)(XT + (16 * w + 4 * g + e) * 72) + 16 * lt + j;
            r[e] = rbf(bf2f(*px) + rbf(acc[lt][e]));
            if (!FINAL) *px = (u16)(__float_as_uint(r[e]) >> 16);
        }
        if (FINAL) {
            *(u32x2*)(OT + (16 * lt + j) * 272 + (16 * w + 4 * g) * 2) = u32x2{pk_bf(r[0], r[1]), pk_bf(r[2], r[3])};
        } else {
            float s = (r[0] + r[1]) + (r[2] + r[3]);
            s += __shfl_xor(s, 16); s += __shfl_xor(s, 32);
            float mean = s * (1.0f / 16.0f), q = 0.f;
#pragma unroll
            for (int e = 0; e < 4; ++e) { float d = r[e] - mean; q = fmaf(d, d, q); }
            q += __shfl_xor(q, 16); q += __shfl_xor(q, 32);
            if (g == 0) ST[w * 32 + 16 * lt + j] = f32x2{mean, q};
        }
    }
    __syncthreads();
    if (FINAL) {
        u32x4 o = *(const u32x4*)(OT + (t >> 4) * 272 + (t & 15) * 16);
        *(u32x4*)(xo + (size_t)(l0 + (t >> 4)) * DM + cq * 128 + (t & 15) * 8) = o;
    } else {
        const char* p = XT + (t >> 2) * 72 + (t & 3) * 16;
        u32x2 a = *(const u32x2*)p, c = *(const u32x2*)(p + 8);
        *(u32x4*)(xo + (size_t)(cq * 128 + (t >> 2)) * SL + l0 + (t & 3) * 8) = u32x4{a[0], a[1], c[0], c[1]};
        if (t < 32) {
            float mean = 0.f, m2 = 0.f;
#pragma unroll
            for (int i = 0; i < 8; ++i) { f32x2 s = ST[i * 32 + t]; mean += s[0]; m2 += s[1]; }
            mean *= 0.125f;
#pragma unroll
            for (int i = 0; i < 8; ++i) { float d = ST[i * 32 + t][0] - mean; m2 = fmaf(16.0f * d, d, m2); }
            stats[cq * SL + l0 + t] = f32x2{mean, m2};
        }
    }
}

extern "C" void kernel_launch(void* const* d_in, const int* in_sizes, int n_in,
                              void* d_out, int out_size, void* d_ws, size_t ws_size,
                              hipStream_t stream) {
    const u16* u = (const u16*)d_in[0];
    const u16* lar = (const u16*)d_in[1];
    const u16* aim = (const u16*)d_in[2];
    const u16* cre = (const u16*)d_in[3];
    const u16* cim = (const u16*)d_in[4];
    const u16* ldt = (const u16*)d_in[5];
    const u16* dsk = (const u16*)d_in[6];
    const u16* wqkv = (const u16*)d_in[8];
    const u16* wo = (const u16*)d_in[9];
    char* ws = (char*)d_ws;
    const size_t MB = 1 << 20;
    u16* xT0 = (u16*)(ws + 0 * MB);
    u16* xT1 = (u16*)(ws + 2 * MB);
    u16* xT2 = (u16*)(ws + 4 * MB);
    u16* xT3 = (u16*)(ws + 6 * MB);
    u16* pqF = (u16*)(ws + 8 * MB);
    u16* Kr = (u16*)(ws + 10 * MB);
    u16* wqF = (u16*)(ws + 13 * MB);
    u16* woF = (u16*)(ws + 15 * MB);
    f32x2* st0 = (f32x2*)(ws + 16 * MB);
    f32x2* st2 = (f32x2*)(ws + 16 * MB + 65536);
    float* Kscale = (float*)(ws + 16 * MB + 196608);
    float* Wsum = (float*)(ws + 16 * MB + 200704);
    float* kvacc = (float*)(ws + 16 * MB + 262144);
    prep_kernel<<<P_TR + P_WQ + P_WO + P_KG, 256, 0, stream>>>(u, lar, aim, cre, cim, ldt, wqkv, wo, xT0, st0, Kr, Kscale,
                                                               wqF, Wsum, woF, kvacc);
    conv_kernel<1><<<256, 256, 0, stream>>>(xT0, st0, Kr, Kscale, dsk, xT1);
    qkv_kernel<<<256, 256, 0, stream>>>(xT1, wqF, Wsum, pqF, kvacc);
    oproj_kernel<0><<<256, 512, 0, stream>>>(xT1, pqF, kvacc, woF, xT2, st2);
    conv_kernel<4><<<256, 256, 0, stream>>>(xT2, st2, Kr, Kscale, dsk, xT3);
    qkv_kernel<<<256, 256, 0, stream>>>(xT3, wqF, Wsum, pqF, kvacc + 32 * KVH);
    oproj_kernel<1><<<256, 512, 0, stream>>>(xT3, pqF, kvacc + 32 * KVH, woF, (u16*)d_out, st2);
}
```

```cpp
#include <hip/hip_runtime.h>
#include <hip/hip_bf16.h>
#include <stdint.h>

typedef unsigned short u16;
typedef unsigned int u32;
typedef __attribute__((ext_vector_type(4))) float f32x4;
typedef __attribute__((ext_vector_type(2))) float f32x2;
typedef __attribute__((ext_vector_type(8))) __bf16 bf16x8;
typedef __attribute__((ext_vector_type(2))) __bf16 bf16x2;
typedef __attribute__((ext_vector_type(8))) _Float16 f16x8;
typedef __attribute__((ext_vector_type(2))) _Float16 f16x2;
typedef __attribute__((ext_vector_type(4))) short s16x4;
typedef __attribute__((ext_vector_type(8))) short s16x8;
typedef __attribute__((ext_vector_type(4))) u32 u32x4;
typedef __attribute__((ext_vector_type(2))) u32 u32x2;

#define DEVI __device__ __forceinline__
#define DBG_ARG
#define DBG_PASS(k)
#define STAMP(i)
#define STAMP_END()

constexpr int SL = 2048;
constexpr int DM = 512;
constexpr int NST = 128;
constexpr int XP = 2176;
constexpr int KRL = 2056;
constexpr int KVH = 272;

DEVI float bf_lo(u32 v) { return __uint_as_float(v << 16); }
DEVI float bf_hi(u32 v) { return __uint_as_float(v & 0xffff0000u); }
DEVI float bf2f(u16 v) { return __uint_as_float(((u32)v) << 16); }
DEVI u32 pk_bf(float a, float b) {
    f32x2 x = {a, b};
    return __builtin_bit_cast(u32, __builtin_convertvector(x, bf16x2));
}
DEVI u32 pk_h(float a, float b) {
    f32x2 x = {a, b};
    return __builtin_bit_cast(u32, __builtin_convertvector(x, f16x2));
}
DEVI float rbf(float a) { return bf_lo(pk_bf(a, 0.f)); }
template <int C> DEVI float dpp_f(float x) {
    return __int_as_float(__builtin_amdgcn_update_dpp(0, __float_as_int(x), C, 0xf, 0xf, true));
}
DEVI float row16_sum(float x) {
    x += dpp_f<0xB1>(x); x += dpp_f<0x4E>(x); x += dpp_f<0x141>(x); x += dpp_f<0x140>(x);
    return x;
}
DEVI float quad_rows_sum(float x) {
    u32x2 a = __builtin_amdgcn_permlane16_swap(__float_as_uint(x), __float_as_uint(x), false, false);
    x = __uint_as_float(a[0]) + __uint_as_float(a[1]);
    u32x2 b = __builtin_amdgcn_permlane32_swap(__float_as_uint(x), __float_as_uint(x), false, false);
    return __uint_as_float(b[0]) + __uint_as_float(b[1]);
}
DEVI float rdl(float x, int l) { return __int_as_float(__builtin_amdgcn_readlane(__float_as_int(x), l)); }
DEVI float wave_sum(float x) {
    x = row16_sum(x);
    return (rdl(x, 0) + rdl(x, 16)) + (rdl(x, 32) + rdl(x, 48));
}
DEVI float wave_max(float x) {
    x = fmaxf(x, dpp_f<0xB1>(x)); x = fmaxf(x, dpp_f<0x4E>(x));
    x = fmaxf(x, dpp_f<0x141>(x)); x = fmaxf(x, dpp_f<0x140>(x));
    return fmaxf(fmaxf(rdl(x, 0), rdl(x, 16)), fmaxf(rdl(x, 32), rdl(x, 48)));
}
DEVI f32x4 mfma_bf(bf16x8 a, bf16x8 b, f32x4 c) { return __builtin_amdgcn_mfma_f32_16x16x32_bf16(a, b, c, 0, 0, 0); }
DEVI f32x4 mfma_h(f16x8 a, f16x8 b, f32x4 c) { return __builtin_amdgcn_mfma_f32_16x16x32_f16(a, b, c, 0, 0, 0); }
DEVI f32x4 mfma_bf16k(s16x4 a, s16x4 b, f32x4 c) { return __builtin_amdgcn_mfma_f32_16x16x16bf16_1k(a, b, c, 0, 0, 0); }
DEVI void lds_barrier() {
    asm volatile("s_waitcnt lgkmcnt(0)" ::: "memory");
    __builtin_amdgcn_s_barrier();
    asm volatile("" ::: "memory");
}
DEVI void ln_scalars(float mean, float var, float& mb, float& rs) {
    mb = rbf(mean);
    float sb = rbf(__builtin_amdgcn_sqrtf(rbf(rbf(var) + 1e-5f)));
    rs = __builtin_amdgcn_rcpf(sb);
}

constexpr int P_TR = 256, P_KG = 512;

__global__ __launch_bounds__(256, 2) void prep_kernel(
    const u16* __restrict__ x, const u16* __restrict__ lar, const u16* __restrict__ aim,
    const u16* __restrict__ cre, const u16* __restrict__ cim, const u16* __restrict__ ldt,
    const u16* __restrict__ wqkv, const u16* __restrict__ wo,
    u16* __restrict__ xT, f32x2* __restrict__ stats, u16* __restrict__ Kr, float* __restrict__ Kscale,
    u16* __restrict__ wqF, float* __restrict__ Wsum, u16* __restrict__ woF, float* __restrict__ kvacc DBG_ARG) {
    STAMP(0);
    __shared__ __attribute__((aligned(16))) char sm[21504];
    const int t = threadIdx.x, lane = t & 63, w = t >> 6, g = lane >> 4, j = lane & 15;
    int b = blockIdx.x;
    if (b < P_TR) {
        const int r0 = ((b & 7) * 32 + (b >> 3)) * 8;
        u32 v[8];
        float pv[8];
#pragma unroll
        for (int i = 0; i < 8; ++i) {
            v[i] = *(const u32*)(x + (size_t)(r0 + i) * DM + 2 * t);
            pv[i] = bf2f(x[(size_t)(r0 + i) * DM]);
        }
        u16* dst = xT + (size_t)(2 * t) * XP + r0;
        *(u32x4*)dst = u32x4{__builtin_amdgcn_perm(v[1], v[0], 0x05040100), __builtin_amdgcn_perm(v[3], v[2], 0x05040100),
                             __builtin_amdgcn_perm(v[5], v[4], 0x05040100), __builtin_amdgcn_perm(v[7], v[6], 0x05040100)};
        *(u32x4*)(dst + XP) = u32x4{__builtin_amdgcn_perm(v[1], v[0], 0x07060302), __builtin_amdgcn_perm(v[3], v[2], 0x07060302),
                                    __builtin_amdgcn_perm(v[5], v[4], 0x07060302), __builtin_amdgcn_perm(v[7], v[6], 0x07060302)};
        float u8[8];
#pragma unroll
        for (int i = 0; i < 8; ++i) {
            const float d0 = bf_lo(v[i]) - pv[i], d1 = bf_hi(v[i]) - pv[i];
            u32x2 sw = __builtin_amdgcn_permlane32_swap(__float_as_uint(d0 + d1), __float_as_uint(fmaf(d0, d0, d1 * d1)), false, false);
            u8[i] = __uint_as_float(sw[0]) + __uint_as_float(sw[1]);
        }
        float* red = (float*)sm;
#pragma unroll
        for (int i = 0; i < 4; ++i) {
            u32x2 sw = __builtin_amdgcn_permlane16_swap(__float_as_uint(u8[i]), __float_as_uint(u8[i + 4]), false, false);
            const float tot = row16_sum(__uint_as_float(sw[0]) + __uint_as_float(sw[1]));
            if (j == 0) red[w * 16 + g * 4 + i] = tot;
        }
        lds_barrier();
        if (t < 8) {
            const float S = (red[t] + red[16 + t]) + (red[32 + t] + red[48 + t]);
            const float Q = (red[8 + t] + red[24 + t]) + (red[40 + t] + red[56 + t]);
            const float p = bf2f(x[(size_t)(r0 + t) * DM]);
            stats[r0 + t] = f32x2{fmaf(S, 1.0f / DM, p), fmaxf(fmaf(-S * (1.0f / DM), S, Q), 0.f)};
        }
        STAMP_END();
        return;
    }
    b -= P_TR;
    {
        const int d = b;
        const u16* pc = cre + d * NST; const u16* pi = cim + d * NST;
        float cm = fmaxf(fabsf(bf2f(pc[lane])) + fabsf(bf2f(pi[lane])),
                         fabsf(bf2f(pc[lane + 64])) + fabsf(bf2f(pi[lane + 64])));
        u32x2 vl[2], va[2], vc[2], vi[2];
#pragma unroll
        for (int ts = 0; ts < 2; ++ts) {
            int n0 = d * NST + 32 * w + 16 * ts + 4 * g;
            vl[ts] = *(const u32x2*)(lar + n0); va[ts] = *(const u32x2*)(aim + n0);
            vc[ts] = *(const u32x2*)(cre + n0); vi[ts] = *(const u32x2*)(cim + n0);
        }
        const float dt = rbf(__expf(bf2f(ldt[d])));
        cm = wave_max(cm);
        const float cinv = cm > 0.f ? 1.0f / cm : 0.f;
        STAMP(2);
        __builtin_amdgcn_sched_barrier(0);
        u16 we[8];
        const bool isq = b < 384;
        const int wnt = (b >> 2) % 6, whp = (b >> 2) / 6;
        const int wcol = (wnt % 3) * DM + (2 * whp + wnt / 3) * 16 + j;
        const int wks = 4 * (b & 3) + w, wct = (b - 384) >> 2, wh2 = 4 * w + (b & 3);
        const char* wbase = (const char*)(isq ? wqkv : wo);
        const u32 woff = 2u * (isq ? (32 * wks + 8 * g) * (3 * DM) + wcol : (32 * wh2 + 4 * g) * DM + wct * 16 + j);
        u16* wdst = (isq ? wqF : woF) + 8u * (u32)((isq ? (whp * 16 + wks) * 6 + wnt : wct * 16 + wh2) * 64 + lane);
#pragma unroll
        for (int jj = 0; jj < 8; ++jj)
            we[jj] = *(const u16*)(wbase + (woff + (u32)(isq ? jj * (6 * DM) : ((jj >> 2) * 16 + (jj & 3)) * (2 * DM))));
        if (!isq && t < 136) kvacc[(b - 384) * 136 + t] = 0.f;
        __builtin_amdgcn_sched_barrier(0);
        f32x4 acc[4];
#pragma unroll
        for (int s = 0; s < 4; ++s) acc[s] = f32x4{0.f, 0.f, 0.f, 0.f};
        const float fj = (float)j;
#pragma unroll
        for (int ts = 0; ts < 2; ++ts) {
            u32x4 fr[8];
#pragma unroll
            for (int e2 = 0; e2 < 2; ++e2) {
                const u32 wl = vl[ts][e2], wa = va[ts][e2], wc = vc[ts][e2], wi = vi[ts][e2];
                const f32x2 fc = f32x2{bf_lo(wc), bf_hi(wc)} * cinv, fi = f32x2{bf_lo(wi), bf_hi(wi)} * cinv;
                const f32x2 nfc = -fc;
                const float s1 = -1.4426950408889634f * dt * fj, s2 = 0.15915494309189535f * dt * fj;
                const u32 ea = pk_bf(__expf(bf_lo(wl)), __expf(bf_hi(wl)));
                float m0 = __builtin_amdgcn_exp2f(bf_lo(ea) * s1), m1 = __builtin_amdgcn_exp2f(bf_hi(ea) * s1);
                float r0 = bf_lo(wa) * s2, r1 = bf_hi(wa) * s2;
                r0 -= rintf(r0); r1 -= rintf(r1);
                f32x2 pr = {m0 * __builtin_amdgcn_cosf(r0), m1 * __builtin_amdgcn_cosf(r1)};
                f32x2 pi_ = {m0 * __builtin_amdgcn_sinf(r0), m1 * __builtin_amdgcn_sinf(r1)};
#pragma unroll
                for (int k = 0; k < 8; ++k) {
                    if (k > 0) {
                        f32x2 nr = pr * pr - pi_ * pi_, ni = (pr + pr) * pi_;
                        pr = nr; pi_ = ni;
                    }
                    f32x2 qr = pr, qi = pi_;
                    if (k == 7) {
                        f32x2 sr = {dpp_f<0x108>(pr[0]), dpp_f<0x108>(pr[1])}, si = {dpp_f<0x108>(pi_[0]), dpp_f<0x108>(pi_[1])};
                        f32x2 tr = sr * sr - si * si, ti = (sr + sr) * si;
                        if (j == 0) { qr = tr; qi = ti; }
                    }
                    if (k < 4) { fr[k][2 * e2] = pk_h(qr[0], qi[0]); fr[k][2 * e2 + 1] = pk_h(qr[1], qi[1]); }
                    else {
                        f32x2 br = fc * qr - fi * qi, bi = nfc * qi - fi * qr;
                        fr[k][2 * e2] = pk_h(br[0], bi[0]); fr[k][2 * e2 + 1] = pk_h(br[1], bi[1]);
                    }
                }
            }
#pragma unroll
            for (int s = 0; s < 4; ++s)
                acc[s] = mfma_h(__builtin_bit_cast(f16x8, fr[s]), __builtin_bit_cast(f16x8, fr[4 + s]), acc[s]);
            if (ts == 0) STAMP(3);
        }
        STAMP(1);
        __builtin_amdgcn_sched_barrier(0);
        float* part = (float*)sm;
        float* tab = part + 4096;
        float* wred = tab + 1024;
#pragma unroll
        for (int s = 0; s < 4; ++s) *(f32x4*)(part + (w * 4 + s) * 256 + j * 16 + 4 * g) = acc[s];
        {
            u32x4 o;
            float sw = 0.f;
#pragma unroll
            for (int jj = 0; jj < 4; ++jj) {
                o[jj] = (u32)we[2 * jj] | ((u32)we[2 * jj + 1] << 16);
                sw += bf2f(we[2 * jj]) + bf2f(we[2 * jj + 1]);
            }
            wred[t] = sw;
            lds_barrier();
            STAMP(4);
            *(u32x4*)wdst = o;
            if (isq && t < 16) {
                float a = 0.f;
#pragma unroll
                for (int i = 0; i < 16; ++i) a += wred[i * 16 + t];
                Wsum[(b & 3) * (3 * DM) + wcol] = a;
            }
        }
#pragma unroll
        for (int s = 0; s < 4; ++s)
            tab[s * 256 + t] = (part[s * 256 + t] + part[(4 + s) * 256 + t]) + (part[(8 + s) * 256 + t] + part[(12 + s) * 256 + t]);
        lds_barrier();
        float kv[8];
        {
            const int lb = 2040 - 8 * t;
            const int sh = lb < 256 ? 0 : (lb < 512 ? 1 : (lb < 1024 ? 2 : 3));
            const int hb = sh ? (1 << (sh - 1)) - 1 : 0, od = sh ? 1 : 0;
#pragma unroll
            for (int e = 0; e < 8; ++e) {
                int l = lb + 7 - e;
                int q = (l + hb + ((l >> sh) & od)) >> sh;
                int idx = sh * 256 + q;
                if (q == 256) idx = sh < 3 ? (sh + 1) * 256 + 128 : 768;
                kv[e] = tab[idx];
            }
        }
        u16* dst = Kr + (size_t)d * KRL;
        *(u32x4*)(dst + 8 * t) = u32x4{pk_h(kv[0], kv[1]), pk_h(kv[2], kv[3]), pk_h(kv[4], kv[5]), pk_h(kv[6], kv[7])};
        if (t == 0) { *(u32x4*)(dst + 2048) = u32x4{0, 0, 0, 0}; Kscale[d] = 2.0f * dt * cm; }
        STAMP_END();
    }
}

constexpr int RCP = 4160;
constexpr int UP = (256 + SL) * 2;
template <int NCT>
__global__ __launch_bounds__(512, 1) void conv_kernel(
    const u16* __restrict__ xT, const f32x2* __restrict__ stats, const u16* __restrict__ Kr,
    const float* __restrict__ Kscale, const u16* __restrict__ dskip, u16* __restrict__ xo DBG_ARG) {
    STAMP(0);
    __shared__ __attribute__((aligned(16))) char sm[8 * RCP + 2 * UP + 16384 + 32768];
    char* RC = sm; char* U = sm + 8 * RCP; char* XU = U + 2 * UP; char* RED = XU + 16384;
    const int t = threadIdx.x, lane = t & 63, g = lane >> 4, r = lane & 15;
    const int w = __builtin_amdgcn_readfirstlane(t >> 6);
    const int c0 = 2 * blockIdx.x;
    u32x2 xv[2];
#pragma unroll
    for (int ch = 0; ch < 2; ++ch) xv[ch] = *(const u32x2*)(xT + (size_t)(c0 + ch) * XP + 4 * t);
    const int kch = t >> 8, kt = t & 255;
    const u32x4 ka = *(const u32x4*)(Kr + (size_t)(c0 + kch) * KRL + 8 * kt);
    const u32x2 kb = *(const u32x2*)(Kr + (size_t)(c0 + kch) * KRL + 8 * kt + 8);
    f32x4 sv[NCT][2];
#pragma unroll
    for (int ct = 0; ct < NCT; ++ct)
#pragma unroll
        for (int i = 0; i < 2; ++i) sv[ct][i] = *(const f32x4*)(stats + ct * SL + 4 * t + 2 * i);
    const float ds[2] = {bf2f(dskip[c0]), bf2f(dskip[c0 + 1])};
    const float ksc = Kscale[c0 + (w >> 2)];
    if (t < 64) *(u32x4*)(U + (t >> 5) * UP + (t & 31) * 16) = u32x4{0, 0, 0, 0};
    else if (t < 80) *(u32x4*)(RC + ((t - 64) >> 1) * RCP + 4096 + (t & 1) * 16) = u32x4{0, 0, 0, 0};
    {
        u32 d0 = ka[0], d1 = ka[1], d2 = ka[2], d3 = ka[3], d4 = kb[0], d5 = kb[1];
        u32 a0 = __builtin_amdgcn_alignbit(d1, d0, 16), a1 = __builtin_amdgcn_alignbit(d2, d1, 16);
        u32 a2 = __builtin_amdgcn_alignbit(d3, d2, 16), a3 = __builtin_amdgcn_alignbit(d4, d3, 16);
        u32 a4 = __builtin_amdgcn_alignbit(d5, d4, 16);
        char* p = RC + kch * 4 * RCP + kt * 16;
        *(u32x4*)(p) = u32x4{d0, d1, d2, d3};
        *(u32x4*)(p + RCP) = u32x4{a0, a1, a2, a3};
        *(u32x4*)(p + 2 * RCP) = u32x4{d1, d2, d3, d4};
        *(u32x4*)(p + 3 * RCP) = u32x4{a1, a2, a3, a4};
    }
    STAMP(1);
    float mb[4], rs[4];
#pragma unroll
    for (int e = 0; e < 4; ++e) {
        float mean, m2;
        if (NCT == 1) { mean = sv[0][e >> 1][(e & 1) * 2]; m2 = sv[0][e >> 1][(e & 1) * 2 + 1]; }
        else {
            mean = 0.f; m2 = 0.f;
#pragma unroll
            for (int ct = 0; ct < NCT; ++ct) { mean += sv[ct][e >> 1][(e & 1) * 2]; m2 += sv[ct][e >> 1][(e & 1) * 2 + 1]; }
            mean *= (1.0f / NCT);
#pragma unroll
            for (int ct = 0; ct < NCT; ++ct) { float dd = sv[ct][e >> 1][(e & 1) * 2] - mean; m2 = fmaf(dd * (float)(DM / NCT), dd, m2); }
        }
        ln_scalars(mean, m2 * (1.0f / DM), mb[e], rs[e]);
    }
#pragma unroll
    for (int ch = 0; ch < 2; ++ch) {
        float xf[4] = {bf_lo(xv[ch][0]), bf_hi(xv[ch][0]), bf_lo(xv[ch][1]), bf_hi(xv[ch][1])};
        u32 d01 = pk_bf(xf[0] - mb[0], xf[1] - mb[1]), d23 = pk_bf(xf[2] - mb[2], xf[3] - mb[3]);
        u32 u01 = pk_bf(bf_lo(d01) * rs[0], bf_hi(d01) * rs[1]), u23 = pk_bf(bf_lo(d23) * rs[2], bf_hi(d23) * rs[3]);
        float uu[4] = {bf_lo(u01), bf_hi(u01), bf_lo(u23), bf_hi(u23)};
        u32 s01 = pk_bf(uu[0] * ds[ch], uu[1] * ds[ch]), s23 = pk_bf(uu[2] * ds[ch], uu[3] * ds[ch]);
        *(u32x2*)(U + ch * UP + 512 + t * 8) = u32x2{pk_h(uu[0], uu[1]), pk_h(uu[2], uu[3])};
        *(u32x4*)(XU + ch * 8192 + t * 16) = u32x4{xv[ch][0], s01, xv[ch][1], s23};
    }
    STAMP(2);
    lds_barrier();
    STAMP(3);
    if (w < 4) {
        const int ch = w >> 1, mh = w & 1;
        f32x4 acc[8];
#pragma unroll
        for (int i = 0; i < 8; ++i) acc[i] = f32x4{0.f, 0.f, 0.f, 0.f};
        const u32 lb = (u32)(uintptr_t)(__attribute__((address_space(3))) char*)sm;
        const u32 fb = lb + (ch * 4 + 3 - (r & 3)) * RCP + 2 * (2028 - 32 * (4 * mh) - 256 * 7 - (r & ~3) + 8 * g) - 192;
        const u32 ub = lb + 8 * RCP + ch * UP + 2 * (256 - 32 * (4 * mh) - 16 + 16 * r + 8 * g) - 192;
        struct CF { u32x2 lo[8], hi[8]; u32x4 b[8]; };
        CF c0f, c1f;
#define C_RDF(F, MU, I) do { \
        asm volatile("ds_read_b64 %0, %1 offset:%2" : "=v"(F.lo[I]) : "v"(fb), "n"(192 - 64 * (MU) + 512 * (7 - (I)))); \
        asm volatile("ds_read_b64 %0, %1 offset:%2" : "=v"(F.hi[I]) : "v"(fb), "n"(200 - 64 * (MU) + 512 * (7 - (I)))); } while (0)
#define C_RDB(F, MU, I) asm volatile("ds_read_b128 %0, %1 offset:%2" : "=v"(F.b[I]) : "v"(ub), "n"(192 - 64 * (MU) + 512 * (I)))
#define C_WAIT(F) do { asm volatile("s_waitcnt lgkmcnt(0)" : "+v"(F.lo[0]), "+v"(F.lo[1]), "+v"(F.lo[2]), "+v"(F.lo[3]), "+v"(F.lo[4]), "+v"(F.lo[5]), \
        "+v"(F.lo[6]), "+v"(F.lo[7]), "+v"(F.hi[0]), "+v"(F.hi[1]), "+v"(F.hi[2]), "+v"(F.hi[3]), "+v"(F.hi[4]), "+v"(F.hi[5]), "+v"(F.hi[6]), "+v"(F.hi[7])); \
        asm volatile("" : "+v"(F.b[0]), "+v"(F.b[1]), "+v"(F.b[2]), "+v"(F.b[3]), "+v"(F.b[4]), "+v"(F.b[5]), "+v"(F.b[6]), "+v"(F.b[7])); } while (0)
#define C_FR(F, I) __builtin_bit_cast(f16x8, u32x4{F.lo[I][0], F.lo[I][1], F.hi[I][0], F.hi[I][1]})
#define C_STEP(CUR, NXT, MUN, PF) do { \
        _Pragma("unroll") for (int a = 0; a < 8; ++a) { \
            _Pragma("unroll") for (int q = 7; q >= 0; --q) { \
                if (q < 8 - a) { \
                    const int slot = a * 8 - a * (a - 1) / 2 + (7 - a - q); \
                    if (slot % 5 == 0) { \
                        __builtin_amdgcn_sched_barrier(0); \
                        if (PF) { C_RDF(NXT, MUN, slot / 5); C_RDB(NXT, MUN, slot / 5); } \
                    } \
                    acc[a + q] = mfma_h(C_FR(CUR, a), __builtin_bit_cast(f16x8, CUR.b[q]), acc[a + q]); \
                } } } \
        __builtin_amdgcn_sched_barrier(0); } while (0)
#pragma unroll
        for (int i = 0; i < 8; ++i) { C_RDF(c0f, 0, i); C_RDB(c0f, 0, i); }
        C_WAIT(c0f);
        __builtin_amdgcn_sched_barrier(0);
        C_STEP(c0f, c1f, 1, true);  C_WAIT(c1f);
        C_STEP(c1f, c0f, 2, true);  C_WAIT(c0f);
        C_STEP(c0f, c1f, 3, true);  C_WAIT(c1f);
        C_STEP(c1f, c0f, 0, false);
#undef C_RDF
#undef C_RDB
#undef C_WAIT
#undef C_FR
#undef C_STEP
        STAMP(4);
#pragma unroll
        for (int i = 0; i < 8; ++i) *(f32x4*)(RED + ((ch * 2 + mh) * 8 + i) * 1024 + lane * 16) = acc[i];
    }
    lds_barrier();
    STAMP(5);
    const int ch = w >> 2;
    u16* dst = xo + (size_t)(c0 + ch) * XP;
#pragma unroll
    for (int i = 0; i < 2; ++i) {
        const int tt = 2 * (w & 3) + i;
        f32x4 y = (*(const f32x4*)(RED + (ch * 16 + tt) * 1024 + lane * 16) + *(const f32x4*)(RED + (ch * 16 + 8 + tt) * 1024 + lane * 16)) * ksc;
        int l = 256 * tt + 16 * r + 4 * g;
        u32x4 xu = *(const u32x4*)(XU + ch * 8192 + l * 4);
        u32 y01 = pk_bf(y[0], y[1]), y23 = pk_bf(y[2], y[3]);
        u32 z01 = pk_bf(bf_lo(y01) + bf_lo(xu[1]), bf_hi(y01) + bf_hi(xu[1])), z23 = pk_bf(bf_lo(y23) + bf_lo(xu[3]), bf_hi(y23) + bf_hi(xu[3]));
        *(u32x2*)(dst + l) = u32x2{pk_bf(bf_lo(xu[0]) + bf_lo(z01), bf_hi(xu[0]) + bf_hi(z01)),
                                   pk_bf(bf_lo(xu[2]) + bf_lo(z23), bf_hi(xu[2]) + bf_hi(z23))};
    }
    STAMP_END();
}

constexpr int QSLOT = 28672, QNS = 5;
DEVI u32 phi2(f32x2 a) {
    const u32 tb = pk_bf(a[0], a[1]);
    const f32x2 tq = {bf_lo(tb), bf_hi(tb)};
    const f32x2 mn = {__builtin_amdgcn_fmed3f(tq[0], -__builtin_inff(), 0.f), __builtin_amdgcn_fmed3f(tq[1], -__builtin_inff(), 0.f)};
    const f32x2 me = mn * 1.4426950408889634f;
    const f32x2 p = f32x2{__builtin_amdgcn_exp2f(me[0]), __builtin_amdgcn_exp2f(me[1])} + (tq - mn);
    const u32 pb = pk_bf(p[0], p[1]);
    const f32x2 p2 = f32x2{bf_lo(pb), bf_hi(pb)} + 1e-6f;
    return pk_bf(p2[0], p2[1]);
}
template <int N> DEVI void wait_vm() { asm volatile("s_waitcnt vmcnt(%0)" ::"n"(N) : "memory"); }

__global__ __launch_bounds__(512, 1) void qkv_kernel(
    const u16* __restrict__ xT, const u16* __restrict__ wqF, const float* __restrict__ Wsum,
    u16* __restrict__ pqF, float* __restrict__ kvacc DBG_ARG) {
    STAMP(0);
    __shared__ __attribute__((aligned(16))) char ring[QNS * QSLOT + 4096];
    float* st = (float*)(ring + QNS * QSLOT);
    float* lnm = st + 256;
    float* lnr = lnm + 128;
    float* wst = lnr + 128;
    const int t = threadIdx.x, lane = t & 63, g = lane >> 4, j = lane & 15;
    const int w = __builtin_amdgcn_readfirstlane(t >> 6), rg = (w >> 1) & 1, hh = w & 1;
    const int xcd = blockIdx.x & 7, jb = blockIdx.x >> 3;
    const int rt = 4 * (xcd >> 1) + (jb & 3), hp = 8 * (xcd & 1) + (jb >> 2);
    f32x4 aq[4], ak[4], av[4];
    char* hand = ring;
    if (w >= 4) {
        const int lw = w - 4;
        const u16* xs[4];
#pragma unroll
        for (int i = 0; i < 4; ++i) {
            int row = 4 * (4 * lw + i) + g, c16 = j;
            int f = (row & 3) | (((row >> 3) & 1) << 2);
            xs[i] = xT + (size_t)row * XP + rt * 128 + ((c16 >> 1) ^ f) * 16 + (c16 & 1) * 8;
        }
        const u16* wsrc = wqF + (size_t)hp * (16 * 6 * 512) + (3 * lw) * 512 + lane * 8;
        auto issue = [&](int kc) {
            char* slot = ring + (kc % QNS) * QSLOT;
#pragma unroll
            for (int i = 0; i < 4; ++i)
                __builtin_amdgcn_global_load_lds((const u32*)(xs[i] + (size_t)kc * 64 * XP), (u32*)(slot + (4 * lw + i) * 1024), 16, 0, 0);
#pragma unroll
            for (int i = 0; i < 3; ++i)
                __builtin_amdgcn_global_load_lds((const u32*)(wsrc + kc * (12 * 512) + i * 512), (u32*)(slot + 16384 + (3 * lw + i) * 1024), 16, 0, 0);
        };
        issue(0); issue(1);
        wait_vm<7>();
        __builtin_amdgcn_s_barrier();
        issue(2);
#pragma unroll
        for (int kc = 1; kc < 8; ++kc) {
            if (kc + 2 < 8) issue(kc + 2);
            if (kc <= 5) wait_vm<14>(); else if (kc == 6) wait_vm<7>(); else wait_vm<0>();
            __builtin_amdgcn_s_barrier();
        }
        STAMP(2);
    } else {
        if (w == 0) {
            const int h2 = lane >= 48 ? 0 : lane;
            if (lane < 48) {
                const float* src = Wsum + (h2 / 16) * DM + 2 * hp * 16 + (h2 & 15);
                constexpr int P = 3 * DM;
                wst[h2] = (src[0] + src[P]) + (src[2 * P] + src[3 * P]);
                wst[48 + h2] = (src[16] + src[P + 16]) + (src[2 * P + 16] + src[3 * P + 16]);
            }
        }
        f32x4 as[4];
#pragma unroll
        for (int i = 0; i < 4; ++i) { aq[i] = ak[i] = av[i] = as[i] = f32x4{0.f, 0.f, 0.f, 0.f}; }
        const u32 ones = 0x3f803f80u;
        const bf16x8 onesv = __builtin_bit_cast(bf16x8, u32x4{ones, ones, ones, ones});
        const int q4 = (lane >> 2) & 3, fl = q4 | ((g & 1) << 2);
        const u32 lbase = (u32)(uintptr_t)(__attribute__((address_space(3))) char*)ring;
        u32 xoff[4];
#pragma unroll
        for (int mt = 0; mt < 4; ++mt) xoff[mt] = lbase + (8 * g + q4) * 256 + (((4 * rg + mt) ^ fl) * 32) + (lane & 3) * 8;
        const u32 woff = lbase + 16384 + hh * 3 * 1024 + lane * 16;
        struct Frag { u32x2 lo[4], hi[4]; u32x4 wq, wk, wv; };
        auto rd = [&](Frag& f, int kc, int ks) {
            const u32 so = (kc % QNS) * QSLOT;
#pragma unroll
            for (int mt = 0; mt < 4; ++mt) {
                u32 a = xoff[mt] + so;
                if (ks == 0) {
                    asm volatile("ds_read_b64_tr_b16 %0, %1" : "=v"(f.lo[mt]) : "v"(a));
                    asm volatile("ds_read_b64_tr_b16 %0, %1 offset:1024" : "=v"(f.hi[mt]) : "v"(a));
                } else {
                    asm volatile("ds_read_b64_tr_b16 %0, %1 offset:8192" : "=v"(f.lo[mt]) : "v"(a));
                    asm volatile("ds_read_b64_tr_b16 %0, %1 offset:9216" : "=v"(f.hi[mt]) : "v"(a));
                }
            }
            u32 a = woff + so;
            if (ks == 0) {
                asm volatile("ds_read_b128 %0, %1" : "=v"(f.wq) : "v"(a));
                asm volatile("ds_read_b128 %0, %1 offset:1024" : "=v"(f.wk) : "v"(a));
                asm volatile("ds_read_b128 %0, %1 offset:2048" : "=v"(f.wv) : "v"(a));
            } else {
                asm volatile("ds_read_b128 %0, %1 offset:6144" : "=v"(f.wq) : "v"(a));
                asm volatile("ds_read_b128 %0, %1 offset:7168" : "=v"(f.wk) : "v"(a));
                asm volatile("ds_read_b128 %0, %1 offset:8192" : "=v"(f.wv) : "v"(a));
            }
        };
#define Q_WAIT(F, N) asm volatile("s_waitcnt lgkmcnt(" #N ")" : "+v"(F.lo[0]), "+v"(F.hi[0]), "+v"(F.lo[1]), "+v"(F.hi[1]), \
        "+v"(F.lo[2]), "+v"(F.hi[2]), "+v"(F.lo[3]), "+v"(F.hi[3]), "+v"(F.wq), "+v"(F.wk), "+v"(F.wv) :: "memory")
        auto mm = [&](const Frag& f) {
            bf16x8 wq = __builtin_bit_cast(bf16x8, f.wq), wk = __builtin_bit_cast(bf16x8, f.wk), wv = __builtin_bit_cast(bf16x8, f.wv);
            bf16x8 xa[4];
#pragma unroll
            for (int mt = 0; mt < 4; ++mt) xa[mt] = __builtin_bit_cast(bf16x8, u32x4{f.lo[mt][0], f.lo[mt][1], f.hi[mt][0], f.hi[mt][1]});
#pragma unroll
            for (int mt = 0; mt < 4; ++mt) {
                aq[mt] = mfma_bf(wq, xa[mt], aq[mt]);
                ak[mt] = mfma_bf(xa[mt], wk, ak[mt]);
                av[mt] = mfma_bf(xa[mt], wv, av[mt]);
            }
            if (hh) {
#pragma unroll
                for (int mt = 0; mt < 4; ++mt) as[mt] = mfma_bf(xa[mt], onesv, as[mt]);
            } else {
#pragma unroll
                for (int mt = 0; mt < 4; ++mt) as[mt] = mfma_bf(xa[mt], xa[mt], as[mt]);
            }
        };
        Frag fa, fb;
        asm volatile("s_waitcnt vmcnt(0) lgkmcnt(0)" ::: "memory");
        __builtin_amdgcn_s_barrier();
        asm volatile("" ::: "memory");
        STAMP(1);
        rd(fa, 0, 0);
#pragma unroll
        for (int kc = 0; kc < 8; ++kc) {
            rd(fb, kc, 1);
            Q_WAIT(fa, 11);
            mm(fa);
            Q_WAIT(fb, 0);
            if (kc < 7) {
                __builtin_amdgcn_s_barrier();
                asm volatile("" ::: "memory");
                STAMP(3 + kc);
                rd(fa, kc + 1, 0);
            }
            mm(fb);
        }
#undef Q_WAIT
        STAMP(10);
#pragma unroll
        for (int mt = 0; mt < 4; ++mt) {
            int rgi = j & 3;
            float v = rgi == 0 ? as[mt][0] : (rgi == 1 ? as[mt][1] : (rgi == 2 ? as[mt][2] : as[mt][3]));
            if (g == (j >> 2)) st[(hh ^ 1) * 128 + rg * 64 + mt * 16 + j] = v;
        }
#pragma unroll
        for (int i = 0; i < 2; ++i) {
            *(f32x4*)(hand + ((w * 6 + 3 * i) * 64 + lane) * 16) = aq[2 + i];
            *(f32x4*)(hand + ((w * 6 + 3 * i + 1) * 64 + lane) * 16) = ak[2 + i];
            *(f32x4*)(hand + ((w * 6 + 3 * i + 2) * 64 + lane) * 16) = av[2 + i];
        }
    }
    lds_barrier();
    if (w >= 4) {
#pragma unroll
        for (int i = 0; i < 2; ++i) {
            aq[i] = *(const f32x4*)(hand + (((w - 4) * 6 + 3 * i) * 64 + lane) * 16);
            ak[i] = *(const f32x4*)(hand + (((w - 4) * 6 + 3 * i + 1) * 64 + lane) * 16);
            av[i] = *(const f32x4*)(hand + (((w - 4) * 6 + 3 * i + 2) * 64 + lane) * 16);
        }
    } else if (t < 128) {
        float sum = st[t], sq = st[128 + t];
        float mean = sum * (1.0f / DM);
        float var = fmaxf(fmaf(-mean, mean, sq * (1.0f / DM)), 0.f);
        float mb, rs;
        ln_scalars(mean, var, mb, rs);
        lnm[t] = mb; lnr[t] = rs;
    }
    lds_barrier();
    STAMP(11);
    const int mt0 = (w >> 2) * 2;
    const f32x4 wsq = *(const f32x4*)(wst + hh * 48 + 4 * g);
    const float wsk = wst[hh * 48 + 16 + j], wsv = wst[hh * 48 + 32 + j];
    u32 pk8[4], vv8[4];
#pragma unroll
    for (int i = 0; i < 2; ++i) {
        const int r0 = rg * 64 + (mt0 + i) * 16;
        const float m1 = lnm[r0 + j], r1 = lnr[r0 + j];
        const u32 q01 = phi2((f32x2{aq[i][0], aq[i][1]} - f32x2{wsq[0], wsq[1]} * m1) * r1);
        const u32 q23 = phi2((f32x2{aq[i][2], aq[i][3]} - f32x2{wsq[2], wsq[3]} * m1) * r1);
        const int lt = rt * 8 + rg * 4 + mt0 + i;
        *(u32x2*)(pqF + ((size_t)(lt * 16 + hp) * 64 + lane) * 8 + hh * 4) = u32x2{q01, q23};
        const f32x4 mr = *(const f32x4*)(lnm + r0 + 4 * g), rr = *(const f32x4*)(lnr + r0 + 4 * g);
        const f32x2 m01 = {mr[0], mr[1]}, m23 = {mr[2], mr[3]}, r01 = {rr[0], rr[1]}, r23 = {rr[2], rr[3]};
        pk8[2 * i] = phi2((f32x2{ak[i][0], ak[i][1]} - m01 * wsk) * r01);
        pk8[2 * i + 1] = phi2((f32x2{ak[i][2], ak[i][3]} - m23 * wsk) * r23);
        const f32x2 v01 = (f32x2{av[i][0], av[i][1]} - m01 * wsv) * r01, v23 = (f32x2{av[i][2], av[i][3]} - m23 * wsv) * r23;
        vv8[2 * i] = pk_bf(v01[0], v01[1]); vv8[2 * i + 1] = pk_bf(v23[0], v23[1]);
    }
    const u32 ones = 0x3f803f80u;
    const bf16x8 pk = __builtin_bit_cast(bf16x8, u32x4{pk8[0], pk8[1], pk8[2], pk8[3]});
    const bf16x8 vv = __builtin_bit_cast(bf16x8, u32x4{vv8[0], vv8[1], vv8[2], vv8[3]});
    const f32x4 z4 = {0.f, 0.f, 0.f, 0.f};
    const f32x4 kvT = mfma_bf(vv, pk, z4);
    const f32x4 zz = mfma_bf(__builtin_bit_cast(bf16x8, u32x4{ones, ones, ones, ones}), pk, z4);
    STAMP(12);
    float* red = (float*)(ring + QSLOT);
    float* rp = red + (hh * 4 + (w >> 1)) * KVH;
#pragma unroll
    for (int e = 0; e < 4; ++e) rp[(4 * g + e) * 16 + j] = kvT[e];
    if (g == 0) rp[256 + j] = zz[0];
    lds_barrier();
    for (int i = t; i < 2 * KVH; i += 512) {
        int h2 = i >= KVH, ii = i - h2 * KVH;
        const float* rq = red + h2 * 4 * KVH + ii;
        unsafeAtomicAdd(kvacc + (2 * hp + h2) * KVH + ii, (rq[0] + rq[KVH]) + (rq[2 * KVH] + rq[3 * KVH]));
    }
    STAMP_END();
}

template <int FINAL>
__global__ __launch_bounds__(512, 1) void oproj_kernel(
    const u16* __restrict__ xT, const u16* __restrict__ pqF, const float* __restrict__ kvacc,
    const u16* __restrict__ woF, u16* __restrict__ xo, f32x2* __restrict__ stats DBG_ARG) {
    STAMP(0);
    __shared__ __attribute__((aligned(16))) char sm[32768 + 128 * 72 + 32 * 272 + 2048];
    char* OF = sm; char* XT = sm + 32768; char* OT = XT + 128 * 72; f32x2* ST = (f32x2*)(OT + 32 * 272);
    const int t = threadIdx.x, lane = t & 63, w = t >> 6, g = lane >> 4, j = lane & 15;
    const int xcd = blockIdx.x & 7, jb = blockIdx.x >> 3;
    const int rt = xcd * 8 + (jb >> 2), cq = jb & 3, l0 = rt * 32;
    u32x4 pq[2][2];
#pragma unroll
    for (int lt = 0; lt < 2; ++lt)
#pragma unroll
        for (int i = 0; i < 2; ++i) pq[lt][i] = *(const u32x4*)(pqF + ((size_t)((2 * rt + lt) * 16 + 2 * w + i) * 64 + lane) * 8);
    f32x4 kf[4], zf[4];
#pragma unroll
    for (int i = 0; i < 4; ++i) {
        kf[i] = *(const f32x4*)(kvacc + (4 * w + i) * KVH + j * 16 + 4 * g);
        zf[i] = *(const f32x4*)(kvacc + (4 * w + i) * KVH + 256 + 4 * g);
    }
    u32x4 xin = *(const u32x4*)(xT + (size_t)(cq * 128 + (t >> 2)) * XP + l0 + (t & 3) * 8);
    bf16x8 wf[16];
#pragma unroll
    for (int ks = 0; ks < 16; ++ks) wf[ks] = *(const bf16x8*)(woF + ((size_t)((cq * 8 + w) * 16 + ks) * 64 + lane) * 8);
    {
        char* p = XT + (t >> 2) * 72 + (t & 3) * 16;
        *(u32x2*)p = u32x2{xin[0], xin[1]}; *(u32x2*)(p + 8) = u32x2{xin[2], xin[3]};
    }
#pragma unroll
    for (int i = 0; i < 2; ++i) {
        u32x4 of[2];
#pragma unroll
        for (int h2 = 0; h2 < 2; ++h2) {
            f32x4 k4 = kf[2 * i + h2], z4 = zf[2 * i + h2];
            s16x4 ka = __builtin_bit_cast(s16x4, u32x2{pk_bf(k4[0], k4[1]), pk_bf(k4[2], k4[3])});
            s16x4 za = __builtin_bit_cast(s16x4, u32x2{pk_bf(z4[0], z4[1]), pk_bf(z4[2], z4[3])});
#pragma unroll
            for (int lt = 0; lt < 2; ++lt) {
                s16x4 b = __builtin_bit_cast(s16x4, u32x2{pq[lt][i][2 * h2], pq[lt][i][2 * h2 + 1]});
                f32x4 zero = {0.f, 0.f, 0.f, 0.f};
                f32x4 num = mfma_bf16k(ka, b, zero), den = mfma_bf16k(za, b, zero);
                float rd = __builtin_amdgcn_rcpf(rbf(rbf(den[0]) + 1e-6f));
                const u32 n01 = pk_bf(num[0], num[1]), n23 = pk_bf(num[2], num[3]);
                const f32x2 o01 = f32x2{bf_lo(n01), bf_hi(n01)} * rd, o23 = f32x2{bf_lo(n23), bf_hi(n23)} * rd;
                of[lt][2 * h2] = pk_bf(o01[0], o01[1]);
                of[lt][2 * h2 + 1] = pk_bf(o23[0], o23[1]);
            }
        }
#pragma unroll
        for (int lt = 0; lt < 2; ++lt) *(u32x4*)(OF + (lt * 16 + 2 * w + i) * 1024 + lane * 16) = of[lt];
    }
    STAMP(1);
    lds_barrier();
    STAMP(2);
    f32x4 acc[2] = {{0.f, 0.f, 0.f, 0.f}, {0.f, 0.f, 0.f, 0.f}};
#pragma unroll
    for (int ks = 0; ks < 16; ++ks)
#pragma unroll
        for (int lt = 0; lt < 2; ++lt)
            acc[lt] = mfma_bf(wf[ks], *(const bf16x8*)(OF + (lt * 16 + ks) * 1024 + lane * 16), acc[lt]);
    STAMP(3);
#pragma unroll
    for (int lt = 0; lt < 2; ++lt) {
        float r[4];
#pragma unroll
        for (int e = 0; e < 4; ++e) {
            u16* px = (u16*)(XT + (16 * w + 4 * g + e) * 72) + 16 * lt + j;
            r[e] = rbf(bf2f(*px) + rbf(acc[lt][e]));
            if (!FINAL) *px = (u16)(__float_as_uint(r[e]) >> 16);
        }
        if (FINAL) {
            *(u32x2*)(OT + (16 * lt + j) * 272 + (16 * w + 4 * g) * 2) = u32x2{pk_bf(r[0], r[1]), pk_bf(r[2], r[3])};
        } else {
            float s = (r[0] + r[1]) + (r[2] + r[3]);
            s = quad_rows_sum(s);
            float mean = s * (1.0f / 16.0f), q = 0.f;
#pragma unroll
            for (int e = 0; e < 4; ++e) { float d = r[e] - mean; q = fmaf(d, d, q); }
            q = quad_rows_sum(q);
            if (g == 0) ST[w * 32 + 16 * lt + j] = f32x2{mean, q};
        }
    }
    lds_barrier();
    if (FINAL) {
        u32x4 o = *(const u32x4*)(OT + (t >> 4) * 272 + (t & 15) * 16);
        *(u32x4*)(xo + (size_t)(l0 + (t >> 4)) * DM + cq * 128 + (t & 15) * 8) = o;
    } else {
        const char* p = XT + (t >> 2) * 72 + (t & 3) * 16;
        u32x2 a = *(const u32x2*)p, c = *(const u32x2*)(p + 8);
        *(u32x4*)(xo + (size_t)(cq * 128 + (t >> 2)) * XP + l0 + (t & 3) * 8) = u32x4{a[0], a[1], c[0], c[1]};
        if (t < 32) {
            float mean = 0.f, m2 = 0.f;
#pragma unroll
            for (int i = 0; i < 8; ++i) { f32x2 s = ST[i * 32 + t]; mean += s[0]; m2 += s[1]; }
            mean *= 0.125f;
#pragma unroll
            for (int i = 0; i < 8; ++i) { float d = ST[i * 32 + t][0] - mean; m2 = fmaf(16.0f * d, d, m2); }
            stats[cq * SL + l0 + t] = f32x2{mean, m2};
        }
    }
    STAMP_END();
}

extern "C" void kernel_launch(void* const* d_in, const int* in_sizes, int n_in,
                              void* d_out, int out_size, void* d_ws, size_t ws_size,
                              hipStream_t stream) {
    const u16* u = (const u16*)d_in[0];
    const u16* lar = (const u16*)d_in[1];
    const u16* aim = (const u16*)d_in[2];
    const u16* cre = (const u16*)d_in[3];
    const u16* cim = (const u16*)d_in[4];
    const u16* ldt = (const u16*)d_in[5];
    const u16* dsk = (const u16*)d_in[6];
    const u16* wqkv = (const u16*)d_in[8];
    const u16* wo = (const u16*)d_in[9];
    char* ws = (char*)d_ws;
    const size_t MB = 1 << 20;
    u16* xT0 = (u16*)(ws + 18 * MB);
    u16* xT1 = (u16*)(ws + 21 * MB);
    u16* xT2 = (u16*)(ws + 24 * MB);
    u16* xT3 = (u16*)(ws + 27 * MB);
    u16* pqF = (u16*)(ws + 8 * MB);
    u16* Kr = (u16*)(ws + 10 * MB);
    u16* wqF = (u16*)(ws + 13 * MB);
    u16* woF = (u16*)(ws + 15 * MB);
    f32x2* st0 = (f32x2*)(ws + 16 * MB);
    f32x2* st2 = (f32x2*)(ws + 16 * MB + 65536);
    float* Kscale = (float*)(ws + 16 * MB + 196608);
    float* Wsum = (float*)(ws + 16 * MB + 200704);
    float* kvacc = (float*)(ws + 16 * MB + 262144);
    prep_kernel<<<P_TR + P_KG, 256, 0, stream>>>(u, lar, aim, cre, cim, ldt, wqkv, wo, xT0, st0, Kr, Kscale,
                                                               wqF, Wsum, woF, kvacc DBG_PASS(0));
    conv_kernel<1><<<256, 512, 0, stream>>>(xT0, st0, Kr, Kscale, dsk, xT1 DBG_PASS(1));
    qkv_kernel<<<256, 512, 0, stream>>>(xT1, wqF, Wsum, pqF, kvacc DBG_PASS(2));
    oproj_kernel<0><<<256, 512, 0, stream>>>(xT1, pqF, kvacc, woF, xT2, st2 DBG_PASS(3));
    conv_kernel<4><<<256, 512, 0, stream>>>(xT2, st2, Kr, Kscale, dsk, xT3 DBG_PASS(4));
    qkv_kernel<<<256, 512, 0, stream>>>(xT3, wqF, Wsum, pqF, kvacc + 32 * KVH DBG_PASS(5));
    oproj_kernel<1><<<256, 512, 0, stream>>>(xT3, pqF, kvacc + 32 * KVH, woF, (u16*)d_out, st2 DBG_PASS(6));
}
```

```cpp
#include <hip/hip_runtime.h>
#include <hip/hip_bf16.h>
#include <stdint.h>

typedef unsigned short u16;
typedef unsigned int u32;
typedef __attribute__((ext_vector_type(4))) float f32x4;
typedef __attribute__((ext_vector_type(2))) float f32x2;
typedef __attribute__((ext_vector_type(8))) __bf16 bf16x8;
typedef __attribute__((ext_vector_type(2))) __bf16 bf16x2;
typedef __attribute__((ext_vector_type(8))) _Float16 f16x8;
typedef __attribute__((ext_vector_type(2))) _Float16 f16x2;
typedef __attribute__((ext_vector_type(4))) short s16x4;
typedef __attribute__((ext_vector_type(8))) short s16x8;
typedef __attribute__((ext_vector_type(4))) u32 u32x4;
typedef __attribute__((ext_vector_type(2))) u32 u32x2;

#define DEVI __device__ __forceinline__
#define DBG_ARG
#define DBG_PASS(k)
#define STAMP(i)
#define STAMP_END()

constexpr int SL = 2048;
constexpr int DM = 512;
constexpr int NST = 128;
constexpr int XP = 2176;
constexpr int KRL = 2056;
constexpr int KVH = 272;

DEVI float bf_lo(u32 v) { return __uint_as_float(v << 16); }
DEVI float bf_hi(u32 v) { return __uint_as_float(v & 0xffff0000u); }
DEVI float bf2f(u16 v) { return __uint_as_float(((u32)v) << 16); }
DEVI u32 pk_bf(float a, float b) {
    f32x2 x = {a, b};
    return __builtin_bit_cast(u32, __builtin_convertvector(x, bf16x2));
}
DEVI u32 pk_h(float a, float b) {
    f32x2 x = {a, b};
    return __builtin_bit_cast(u32, __builtin_convertvector(x, f16x2));
}
DEVI float rbf(float a) { return bf_lo(pk_bf(a, 0.f)); }
template <int C> DEVI float dpp_f(float x) {
    return __int_as_float(__builtin_amdgcn_update_dpp(0, __float_as_int(x), C, 0xf, 0xf, true));
}
DEVI float row16_sum(float x) {
    x += dpp_f<0xB1>(x); x += dpp_f<0x4E>(x); x += dpp_f<0x141>(x); x += dpp_f<0x140>(x);
    return x;
}
DEVI float quad_rows_sum(float x) {
    u32x2 a = __builtin_amdgcn_permlane16_swap(__float_as_uint(x), __float_as_uint(x), false, false);
    x = __uint_as_float(a[0]) + __uint_as_float(a[1]);
    u32x2 b = __builtin_amdgcn_permlane32_swap(__float_as_uint(x), __float_as_uint(x), false, false);
    return __uint_as_float(b[0]) + __uint_as_float(b[1]);
}
DEVI float rdl(float x, int l) { return __int_as_float(__builtin_amdgcn_readlane(__float_as_int(x), l)); }
DEVI float wave_sum(float x) {
    x = row16_sum(x);
    return (rdl(x, 0) + rdl(x, 16)) + (rdl(x, 32) + rdl(x, 48));
}
DEVI float wave_max(float x) {
    x = fmaxf(x, dpp_f<0xB1>(x)); x = fmaxf(x, dpp_f<0x4E>(x));
    x = fmaxf(x, dpp_f<0x141>(x)); x = fmaxf(x, dpp_f<0x140>(x));
    return fmaxf(fmaxf(rdl(x, 0), rdl(x, 16)), fmaxf(rdl(x, 32), rdl(x, 48)));
}
DEVI f32x4 mfma_bf(bf16x8 a, bf16x8 b, f32x4 c) { return __builtin_amdgcn_mfma_f32_16x16x32_bf16(a, b, c, 0, 0, 0); }
DEVI f32x4 mfma_h(f16x8 a, f16x8 b, f32x4 c) { return __builtin_amdgcn_mfma_f32_16x16x32_f16(a, b, c, 0, 0, 0); }
DEVI f32x4 mfma_bf16k(s16x4 a, s16x4 b, f32x4 c) { return __builtin_amdgcn_mfma_f32_16x16x16bf16_1k(a, b, c, 0, 0, 0); }
#define WT 1
DEVI void st16(void* p, u32x4 v) {
    asm volatile("global_store_dwordx4 %0, %1, off sc1\n\ts_nop 1" ::"v"(p), "v"(v) : "memory");
}
DEVI void st8(void* p, u32x2 v) {
    __hip_atomic_store((unsigned long long*)p, __builtin_bit_cast(unsigned long long, v), __ATOMIC_RELAXED, __HIP_MEMORY_SCOPE_AGENT);
}
DEVI void lds_barrier() {
    asm volatile("s_waitcnt lgkmcnt(0)" ::: "memory");
    __builtin_amdgcn_s_barrier();
    asm volatile("" ::: "memory");
}
DEVI void ln_scalars(float mean, float var, float& mb, float& rs) {
    mb = rbf(mean);
    float sb = rbf(__builtin_amdgcn_sqrtf(rbf(rbf(var) + 1e-5f)));
    rs = __builtin_amdgcn_rcpf(sb);
}

__global__ __launch_bounds__(768, 1) void prep_kernel(
    const u16* __restrict__ x, const u16* __restrict__ lar, const u16* __restrict__ aim,
    const u16* __restrict__ cre, const u16* __restrict__ cim, const u16* __restrict__ ldt,
    const u16* __restrict__ wqkv, const u16* __restrict__ wo,
    u16* __restrict__ xT, f32x2* __restrict__ stats, u16* __restrict__ Kr, float* __restrict__ Kscale,
    u16* __restrict__ wqF, float* __restrict__ Wsum, u16* __restrict__ woF, float* __restrict__ kvacc DBG_ARG) {
    STAMP(0);
    __shared__ __attribute__((aligned(16))) char smem[82176];
    const int role = __builtin_amdgcn_readfirstlane(threadIdx.x >> 8);
    const int t = threadIdx.x & 255, lane = t & 63, w = __builtin_amdgcn_readfirstlane(t >> 6), g = lane >> 4, j = lane & 15;
    int b = blockIdx.x;
    if (role == 0) {
        char* sm = smem + 2 * 24576;
        const int r0 = ((b & 7) * 32 + (b >> 3)) * 8;
        u32 v[8];
        float pv[8];
#pragma unroll
        for (int i = 0; i < 8; ++i) {
            v[i] = *(const u32*)(x + (size_t)(r0 + i) * DM + 2 * t);
            pv[i] = bf2f(x[(size_t)(r0 + i) * DM]);
        }
        u16* dst = xT + (size_t)(2 * t) * XP + r0;
        *(u32x4*)dst = u32x4{__builtin_amdgcn_perm(v[1], v[0], 0x05040100), __builtin_amdgcn_perm(v[3], v[2], 0x05040100),
                             __builtin_amdgcn_perm(v[5], v[4], 0x05040100), __builtin_amdgcn_perm(v[7], v[6], 0x05040100)};
        *(u32x4*)(dst + XP) = u32x4{__builtin_amdgcn_perm(v[1], v[0], 0x07060302), __builtin_amdgcn_perm(v[3], v[2], 0x07060302),
                                    __builtin_amdgcn_perm(v[5], v[4], 0x07060302), __builtin_amdgcn_perm(v[7], v[6], 0x07060302)};
        float u8[8];
#pragma unroll
        for (int i = 0; i < 8; ++i) {
            const float d0 = bf_lo(v[i]) - pv[i], d1 = bf_hi(v[i]) - pv[i];
            u32x2 sw = __builtin_amdgcn_permlane32_swap(__float_as_uint(d0 + d1), __float_as_uint(fmaf(d0, d0, d1 * d1)), false, false);
            u8[i] = __uint_as_float(sw[0]) + __uint_as_float(sw[1]);
        }
        float* red = (float*)sm;
#pragma unroll
        for (int i = 0; i < 4; ++i) {
            u32x2 sw = __builtin_amdgcn_permlane16_swap(__float_as_uint(u8[i]), __float_as_uint(u8[i + 4]), false, false);
            const float tot = row16_sum(__uint_as_float(sw[0]) + __uint_as_float(sw[1]));
            if (j == 0) red[w * 16 + g * 4 + i] = tot;
        }
        lds_barrier();
        if (t < 8) {
            const float S = (red[t] + red[16 + t]) + (red[32 + t] + red[48 + t]);
            const float Q = (red[8 + t] + red[24 + t]) + (red[40 + t] + red[56 + t]);
            const float p = bf2f(x[(size_t)(r0 + t) * DM]);
            stats[r0 + t] = f32x2{fmaf(S, 1.0f / DM, p), fmaxf(fmaf(-S * (1.0f / DM), S, Q), 0.f)};
        }
        lds_barrier();
        STAMP_END();
        return;
    }
    b = 2 * b + role - 1;
    char* sm = smem + (role - 1) * 24576;
    {
        const int d = b;
        const u16* pc = cre + d * NST; const u16* pi = cim + d * NST;
        float cm = fmaxf(fabsf(bf2f(pc[lane])) + fabsf(bf2f(pi[lane])),
                         fabsf(bf2f(pc[lane + 64])) + fabsf(bf2f(pi[lane + 64])));
        const int ns = d * NST + 32 * w + (lane & 31);
        const u16 pl = lar[ns], pa = aim[ns], pcr = cre[ns], pci = cim[ns];
        const float dt = rbf(__expf(bf2f(ldt[d])));
        cm = wave_max(cm);
        const float cinv = cm > 0.f ? 1.0f / cm : 0.f;
        float* spar = (float*)(sm + 22528) + w * 128;
        if (lane < 32) {
            spar[lane] = -1.4426950408889634f * dt * rbf(__expf(bf2f(pl)));
            spar[32 + lane] = 0.15915494309189535f * dt * bf2f(pa);
            spar[64 + lane] = bf2f(pcr) * cinv;
            spar[96 + lane] = bf2f(pci) * cinv;
        }
        STAMP(2);
        __builtin_amdgcn_sched_barrier(0);
        u16 we[8];
        const bool isq = b < 384;
        const int wnt = (b >> 2) % 6, whp = (b >> 2) / 6;
        const int wcol = (wnt % 3) * DM + (2 * whp + wnt / 3) * 16 + j;
        const int wks = 4 * (b & 3) + w, wct = (b - 384) >> 2, wh2 = 4 * w + (b & 3);
        const char* wbase = (const char*)(isq ? wqkv : wo);
        const u32 woff = 2u * (isq ? (32 * wks + 8 * g) * (3 * DM) + wcol : (32 * wh2 + 4 * g) * DM + wct * 16 + j);
        u16* wdst = (isq ? wqF : woF) + 8u * (u32)((isq ? (whp * 16 + wks) * 6 + wnt : wct * 16 + wh2) * 64 + lane);
#pragma unroll
        for (int jj = 0; jj < 8; ++jj)
            we[jj] = *(const u16*)(wbase + (woff + (u32)(isq ? jj * (6 * DM) : ((jj >> 2) * 16 + (jj & 3)) * (2 * DM))));
        if (!isq && t < 136) kvacc[(b - 384) * 136 + t] = 0.f;
        __builtin_amdgcn_sched_barrier(0);
        f32x4 acc[4];
#pragma unroll
        for (int s = 0; s < 4; ++s) acc[s] = f32x4{0.f, 0.f, 0.f, 0.f};
        const float fj = (float)j;
#pragma unroll
        for (int ts = 0; ts < 2; ++ts) {
            u32x4 fr[8];
            f32x4 pq4[4];
#pragma unroll
            for (int i = 0; i < 4; ++i) pq4[i] = *(const f32x4*)(spar + 32 * i + 16 * ts + 4 * g);
#pragma unroll
            for (int e2 = 0; e2 < 2; ++e2) {
                const f32x2 ra = {pq4[0][2 * e2], pq4[0][2 * e2 + 1]}, rb = {pq4[1][2 * e2], pq4[1][2 * e2 + 1]};
                const f32x2 fc = {pq4[2][2 * e2], pq4[2][2 * e2 + 1]}, fi = {pq4[3][2 * e2], pq4[3][2 * e2 + 1]};
                const f32x2 nfc = -fc;
                const f32x2 me = ra * fj;
                f32x2 rr = rb * fj;
                rr -= f32x2{rintf(rr[0]), rintf(rr[1])};
                const f32x2 mm = {__builtin_amdgcn_exp2f(me[0]), __builtin_amdgcn_exp2f(me[1])};
                f32x2 pr = mm * f32x2{__builtin_amdgcn_cosf(rr[0]), __builtin_amdgcn_cosf(rr[1])};
                f32x2 pi_ = mm * f32x2{__builtin_amdgcn_sinf(rr[0]), __builtin_amdgcn_sinf(rr[1])};
#pragma unroll
                for (int k = 0; k < 8; ++k) {
                    if (k > 0) {
                        f32x2 nr = pr * pr - pi_ * pi_, ni = (pr + pr) * pi_;
                        pr = nr; pi_ = ni;
                    }
                    f32x2 qr = pr, qi = pi_;
                    if (k == 7) {
                        f32x2 sr = {dpp_f<0x108>(pr[0]), dpp_f<0x108>(pr[1])}, si = {dpp_f<0x108>(pi_[0]), dpp_f<0x108>(pi_[1])};
                        f32x2 tr = sr * sr - si * si, ti = (sr + sr) * si;
                        if (j == 0) { qr = tr; qi = ti; }
                    }
                    if (k < 4) { fr[k][2 * e2] = pk_h(qr[0], qi[0]); fr[k][2 * e2 + 1] = pk_h(qr[1], qi[1]); }
                    else {
                        f32x2 br = fc * qr - fi * qi, bi = nfc * qi - fi * qr;
                        fr[k][2 * e2] = pk_h(br[0], bi[0]); fr[k][2 * e2 + 1] = pk_h(br[1], bi[1]);
                    }
                }
            }
#pragma unroll
            for (int s = 0; s < 4; ++s)
                acc[s] = mfma_h(__builtin_bit_cast(f16x8, fr[s]), __builtin_bit_cast(f16x8, fr[4 + s]), acc[s]);
            if (ts == 0) STAMP(3);
        }
        STAMP(1);
        __builtin_amdgcn_sched_barrier(0);
        float* part = (float*)sm;
        float* tab = part + 4096;
        float* wred = tab + 1280;
#pragma unroll
        for (int s = 0; s < 4; ++s) *(f32x4*)(part + (w * 4 + s) * 256 + j * 16 + 4 * g) = acc[s];
        {
            u32x4 o;
            float sw = 0.f;
#pragma unroll
            for (int jj = 0; jj < 4; ++jj) {
                o[jj] = (u32)we[2 * jj] | ((u32)we[2 * jj + 1] << 16);
                sw += bf2f(we[2 * jj]) + bf2f(we[2 * jj + 1]);
            }
            wred[t] = sw;
            lds_barrier();
            STAMP(4);
            st16(wdst, o);
            if (isq && t < 16) {
                float a = 0.f;
#pragma unroll
                for (int i = 0; i < 16; ++i) a += wred[i * 16 + t];
                Wsum[(b & 3) * (3 * DM) + wcol] = a;
            }
        }
#pragma unroll
        for (int s = 0; s < 4; ++s) {
            const float v = (part[s * 256 + t] + part[(4 + s) * 256 + t]) + (part[(8 + s) * 256 + t] + part[(12 + s) * 256 + t]);
            tab[s * 256 + t] = v;
            if (s == 3 && t == 0) tab[1152] = v;
        }
        lds_barrier();
        float kv[8];
        {
            const int lb = 2040 - 8 * t;
            const bool big = lb >= 128;
#pragma unroll
            for (int e = 0; e < 8; e += 2) {
                const int l = lb + 7 - e;
                const u32 pb = pk_bf((float)l, (float)(l - 1));
                const u32 h0 = pb & 0xffffu, h1 = pb >> 16;
                kv[e] = tab[big ? (int)(h0 + (h0 & 0x7f80u)) - 34176 : l];
                kv[e + 1] = tab[big ? (int)(h1 + (h1 & 0x7f80u)) - 34176 : l - 1];
            }
        }
        u16* dst = Kr + (size_t)d * KRL;
        st16(dst + 8 * t, u32x4{pk_h(kv[0], kv[1]), pk_h(kv[2], kv[3]), pk_h(kv[4], kv[5]), pk_h(kv[6], kv[7])});
        if (t == 0) { *(u32x4*)(dst + 2048) = u32x4{0, 0, 0, 0}; Kscale[d] = 2.0f * dt * cm; }
        STAMP_END();
    }
}

constexpr int RCP = 4160;
constexpr int UP = (256 + SL) * 2;
template <int NCT>
__global__ __launch_bounds__(512, 1) void conv_kernel(
    const u16* __restrict__ xT, const f32x2* __restrict__ stats, const u16* __restrict__ Kr,
    const float* __restrict__ Kscale, const u16* __restrict__ dskip, u16* __restrict__ xo DBG_ARG) {
    STAMP(0);
    __shared__ __attribute__((aligned(16))) char sm[8 * RCP + 2 * UP + 16384 + 32768];
    char* RC = sm; char* U = sm + 8 * RCP; char* XU = U + 2 * UP; char* RED = XU + 16384;
    const int t = threadIdx.x, lane = t & 63, g = lane >> 4, r = lane & 15;
    const int w = __builtin_amdgcn_readfirstlane(t >> 6);
    const int c0 = 2 * blockIdx.x;
    u32x2 xv[2];
#pragma unroll
    for (int ch = 0; ch < 2; ++ch) xv[ch] = *(const u32x2*)(xT + (size_t)(c0 + ch) * XP + 4 * t);
    const int kch = t >> 8, kt = t & 255;
    const u32x4 ka = *(const u32x4*)(Kr + (size_t)(c0 + kch) * KRL + 8 * kt);
    const u32x2 kb = *(const u32x2*)(Kr + (size_t)(c0 + kch) * KRL + 8 * kt + 8);
    f32x4 sv[NCT][2];
#pragma unroll
    for (int ct = 0; ct < NCT; ++ct)
#pragma unroll
        for (int i = 0; i < 2; ++i) sv[ct][i] = *(const f32x4*)(stats + ct * SL + 4 * t + 2 * i);
    const float ds[2] = {bf2f(dskip[c0]), bf2f(dskip[c0 + 1])};
    const float ksc = Kscale[c0 + (w >> 2)];
    if (t < 64) *(u32x4*)(U + (t >> 5) * UP + (t & 31) * 16) = u32x4{0, 0, 0, 0};
    else if (t < 80) *(u32x4*)(RC + ((t - 64) >> 1) * RCP + 4096 + (t & 1) * 16) = u32x4{0, 0, 0, 0};
    {
        u32 d0 = ka[0], d1 = ka[1], d2 = ka[2], d3 = ka[3], d4 = kb[0], d5 = kb[1];
        u32 a0 = __builtin_amdgcn_alignbit(d1, d0, 16), a1 = __builtin_amdgcn_alignbit(d2, d1, 16);
        u32 a2 = __builtin_amdgcn_alignbit(d3, d2, 16), a3 = __builtin_amdgcn_alignbit(d4, d3, 16);
        u32 a4 = __builtin_amdgcn_alignbit(d5, d4, 16);
        char* p = RC + kch * 4 * RCP + kt * 16;
        *(u32x4*)(p) = u32x4{d0, d1, d2, d3};
        *(u32x4*)(p + RCP) = u32x4{a0, a1, a2, a3};
        *(u32x4*)(p + 2 * RCP) = u32x4{d1, d2, d3, d4};
        *(u32x4*)(p + 3 * RCP) = u32x4{a1, a2, a3, a4};
    }
    STAMP(1);
    float mb[4], rs[4];
#pragma unroll
    for (int e = 0; e < 4; ++e) {
        float mean, m2;
        if (NCT == 1) { mean = sv[0][e >> 1][(e & 1) * 2]; m2 = sv[0][e >> 1][(e & 1) * 2 + 1]; }
        else {
            mean = 0.f; m2 = 0.f;
#pragma unroll
            for (int ct = 0; ct < NCT; ++ct) { mean += sv[ct][e >> 1][(e & 1) * 2]; m2 += sv[ct][e >> 1][(e & 1) * 2 + 1]; }
            mean *= (1.0f / NCT);
#pragma unroll
            for (int ct = 0; ct < NCT; ++ct) { float dd = sv[ct][e >> 1][(e & 1) * 2] - mean; m2 = fmaf(dd * (float)(DM / NCT), dd, m2); }
        }
        ln_scalars(mean, m2 * (1.0f / DM), mb[e], rs[e]);
    }
#pragma unroll
    for (int ch = 0; ch < 2; ++ch) {
        float xf[4] = {bf_lo(xv[ch][0]), bf_hi(xv[ch][0]), bf_lo(xv[ch][1]), bf_hi(xv[ch][1])};
        u32 d01 = pk_bf(xf[0] - mb[0], xf[1] - mb[1]), d23 = pk_bf(xf[2] - mb[2], xf[3] - mb[3]);
        u32 u01 = pk_bf(bf_lo(d01) * rs[0], bf_hi(d01) * rs[1]), u23 = pk_bf(bf_lo(d23) * rs[2], bf_hi(d23) * rs[3]);
        float uu[4] = {bf_lo(u01), bf_hi(u01), bf_lo(u23), bf_hi(u23)};
        u32 s01 = pk_bf(uu[0] * ds[ch], uu[1] * ds[ch]), s23 = pk_bf(uu[2] * ds[ch], uu[3] * ds[ch]);
        *(u32x2*)(U + ch * UP + 512 + t * 8) = u32x2{pk_h(uu[0], uu[1]), pk_h(uu[2], uu[3])};
        *(u32x4*)(XU + ch * 8192 + t * 16) = u32x4{xv[ch][0], s01, xv[ch][1], s23};
    }
    STAMP(2);
    lds_barrier();
    STAMP(3);
    if (w < 4) {
        const int ch = w >> 1, mh = w & 1;
        f32x4 acc[8];
#pragma unroll
        for (int i = 0; i < 8; ++i) acc[i] = f32x4{0.f, 0.f, 0.f, 0.f};
        const u32 lb = (u32)(uintptr_t)(__attribute__((address_space(3))) char*)sm;
        const u32 fb = lb + (ch * 4 + 3 - (r & 3)) * RCP + 2 * (2028 - 32 * (4 * mh) - 256 * 7 - (r & ~3) + 8 * g) - 192;
        const u32 ub = lb + 8 * RCP + ch * UP + 2 * (256 - 32 * (4 * mh) - 16 + 16 * r + 8 * g) - 192;
        struct CF { u32x2 lo[8], hi[8]; u32x4 b[8]; };
        CF c0f, c1f;
#define C_RDF(F, MU, I) do { \
        asm volatile("ds_read_b64 %0, %1 offset:%2" : "=v"(F.lo[I]) : "v"(fb), "n"(192 - 64 * (MU) + 512 * (7 - (I)))); \
        asm volatile("ds_read_b64 %0, %1 offset:%2" : "=v"(F.hi[I]) : "v"(fb), "n"(200 - 64 * (MU) + 512 * (7 - (I)))); } while (0)
#define C_RDB(F, MU, I) asm volatile("ds_read_b128 %0, %1 offset:%2" : "=v"(F.b[I]) : "v"(ub), "n"(192 - 64 * (MU) + 512 * (I)))
#define C_WAIT(F) do { asm volatile("s_waitcnt lgkmcnt(0)" : "+v"(F.lo[0]), "+v"(F.lo[1]), "+v"(F.lo[2]), "+v"(F.lo[3]), "+v"(F.lo[4]), "+v"(F.lo[5]), \
        "+v"(F.lo[6]), "+v"(F.lo[7]), "+v"(F.hi[0]), "+v"(F.hi[1]), "+v"(F.hi[2]), "+v"(F.hi[3]), "+v"(F.hi[4]), "+v"(F.hi[5]), "+v"(F.hi[6]), "+v"(F.hi[7])); \
        asm volatile("" : "+v"(F.b[0]), "+v"(F.b[1]), "+v"(F.b[2]), "+v"(F.b[3]), "+v"(F.b[4]), "+v"(F.b[5]), "+v"(F.b[6]), "+v"(F.b[7])); } while (0)
#define C_FR(F, I) __builtin_bit_cast(f16x8, u32x4{F.lo[I][0], F.lo[I][1], F.hi[I][0], F.hi[I][1]})
#define C_STEP(CUR, NXT, MUN, PF) do { \
        _Pragma("unroll") for (int a = 0; a < 8; ++a) { \
            _Pragma("unroll") for (int q = 7; q >= 0; --q) { \
                if (q < 8 - a) { \
                    const int slot = a * 8 - a * (a - 1) / 2 + (7 - a - q); \
                    if (slot % 5 == 0) { \
                        __builtin_amdgcn_sched_barrier(0); \
                        if (PF) { C_RDF(NXT, MUN, slot / 5); C_RDB(NXT, MUN, slot / 5); } \
                    } \
                    acc[a + q] = mfma_h(C_FR(CUR, a), __builtin_bit_cast(f16x8, CUR.b[q]), acc[a + q]); \
                } } } \
        __builtin_amdgcn_sched_barrier(0); } while (0)
#pragma unroll
        for (int i = 0; i < 8; ++i) { C_RDF(c0f, 0, i); C_RDB(c0f, 0, i); }
        C_WAIT(c0f);
        __builtin_amdgcn_sched_barrier(0);
        C_STEP(c0f, c1f, 1, true);  C_WAIT(c1f);
        C_STEP(c1f, c0f, 2, true);  C_WAIT(c0f);
        C_STEP(c0f, c1f, 3, true);  C_WAIT(c1f);
        C_STEP(c1f, c0f, 0, false);
#undef C_RDF
#undef C_RDB
#undef C_WAIT
#undef C_FR
#undef C_STEP
        STAMP(4);
#pragma unroll
        for (int i = 0; i < 8; ++i) *(f32x4*)(RED + ((ch * 2 + mh) * 8 + i) * 1024 + lane * 16) = acc[i];
    }
    lds_barrier();
    STAMP(5);
    const int ch = w >> 2;
    u16* dst = xo + (size_t)(c0 + ch) * XP;
#pragma unroll
    for (int i = 0; i < 2; ++i) {
        const int tt = 2 * (w & 3) + i;
        f32x4 y = (*(const f32x4*)(RED + (ch * 16 + tt) * 1024 + lane * 16) + *(const f32x4*)(RED + (ch * 16 + 8 + tt) * 1024 + lane * 16)) * ksc;
        int l = 256 * tt + 16 * r + 4 * g;
        u32x4 xu = *(const u32x4*)(XU + ch * 8192 + l * 4);
        u32 y01 = pk_bf(y[0], y[1]), y23 = pk_bf(y[2], y[3]);
        u32 z01 = pk_bf(bf_lo(y01) + bf_lo(xu[1]), bf_hi(y01) + bf_hi(xu[1])), z23 = pk_bf(bf_lo(y23) + bf_lo(xu[3]), bf_hi(y23) + bf_hi(xu[3]));
        st8(dst + l, u32x2{pk_bf(bf_lo(xu[0]) + bf_lo(z01), bf_hi(xu[0]) + bf_hi(z01)),
                           pk_bf(bf_lo(xu[2]) + bf_lo(z23), bf_hi(xu[2]) + bf_hi(z23))});
    }
    STAMP_END();
}

constexpr int QSLOT = 28672, QNS = 5;
DEVI u32 phi2(f32x2 a) {
    const u32 tb = pk_bf(a[0], a[1]);
    const f32x2 tq = {bf_lo(tb), bf_hi(tb)};
    const f32x2 mn = {__builtin_amdgcn_fmed3f(tq[0], -__builtin_inff(), 0.f), __builtin_amdgcn_fmed3f(tq[1], -__builtin_inff(), 0.f)};
    const f32x2 me = mn * 1.4426950408889634f;
    const f32x2 p = f32x2{__builtin_amdgcn_exp2f(me[0]), __builtin_amdgcn_exp2f(me[1])} + (tq - mn);
    const u32 pb = pk_bf(p[0], p[1]);
    const f32x2 p2 = f32x2{bf_lo(pb), bf_hi(pb)} + 1e-6f;
    return pk_bf(p2[0], p2[1]);
}
template <int N> DEVI void wait_vm() { asm volatile("s_waitcnt vmcnt(%0)" ::"n"(N) : "memory"); }

__global__ __launch_bounds__(512, 1) void qkv_kernel(
    const u16* __restrict__ xT, const u16* __restrict__ wqF, const float* __restrict__ Wsum,
    u16* __restrict__ pqF, float* __restrict__ kvacc DBG_ARG) {
    STAMP(0);
    __shared__ __attribute__((aligned(16))) char ring[QNS * QSLOT + 4096];
    float* lnm = (float*)(ring + QNS * QSLOT);
    float* lnr = lnm + 128;
    float* wst = lnr + 128;
    const int t = threadIdx.x, lane = t & 63, g = lane >> 4, j = lane & 15;
    const int w = __builtin_amdgcn_readfirstlane(t >> 6), rg = (w >> 1) & 1, hh = w & 1;
    const int xcd = blockIdx.x & 7, jb = blockIdx.x >> 3;
    const int rt = 4 * (xcd >> 1) + (jb & 3), hp = 8 * (xcd & 1) + (jb >> 2);
    f32x4 aq[4], ak[4], av[4];
    char* hand = ring;
    if (w >= 4) {
        const int lw = w - 4;
        const u16* xs[4];
#pragma unroll
        for (int i = 0; i < 4; ++i) {
            int row = 4 * (4 * lw + i) + g, c16 = j;
            int f = (row & 3) | (((row >> 3) & 1) << 2);
            xs[i] = xT + (size_t)row * XP + rt * 128 + ((c16 >> 1) ^ f) * 16 + (c16 & 1) * 8;
        }
        const u16* wsrc = wqF + (size_t)hp * (16 * 6 * 512) + (3 * lw) * 512 + lane * 8;
        auto issue = [&](int kc) {
            char* slot = ring + (kc % QNS) * QSLOT;
#pragma unroll
            for (int i = 0; i < 4; ++i)
                __builtin_amdgcn_global_load_lds((const u32*)(xs[i] + (size_t)kc * 64 * XP), (u32*)(slot + (4 * lw + i) * 1024), 16, 0, 0);
#pragma unroll
            for (int i = 0; i < 3; ++i)
                __builtin_amdgcn_global_load_lds((const u32*)(wsrc + kc * (12 * 512) + i * 512), (u32*)(slot + 16384 + (3 * lw + i) * 1024), 16, 0, 0);
        };
        issue(0); issue(1);
        wait_vm<7>();
        __builtin_amdgcn_s_barrier();
        issue(2);
#pragma unroll
        for (int kc = 1; kc < 8; ++kc) {
            if (kc + 2 < 8) issue(kc + 2);
            if (kc <= 5) wait_vm<14>(); else if (kc == 6) wait_vm<7>(); else wait_vm<0>();
            __builtin_amdgcn_s_barrier();
        }
        STAMP(2);
    } else {
        if (w == 0) {
            const int h2 = lane >= 48 ? 0 : lane;
            if (lane < 48) {
                const float* src = Wsum + (h2 / 16) * DM + 2 * hp * 16 + (h2 & 15);
                constexpr int P = 3 * DM;
                wst[h2] = (src[0] + src[P]) + (src[2 * P] + src[3 * P]);
                wst[48 + h2] = (src[16] + src[P + 16]) + (src[2 * P + 16] + src[3 * P + 16]);
            }
        }
        f32x4 as[4];
#pragma unroll
        for (int i = 0; i < 4; ++i) { aq[i] = ak[i] = av[i] = as[i] = f32x4{0.f, 0.f, 0.f, 0.f}; }
        const u32 ones = 0x3f803f80u;
        const bf16x8 onesv = __builtin_bit_cast(bf16x8, u32x4{ones, ones, ones, ones});
        const int q4 = (lane >> 2) & 3, fl = q4 | ((g & 1) << 2);
        const u32 lbase = (u32)(uintptr_t)(__attribute__((address_space(3))) char*)ring;
        u32 xoff[4];
#pragma unroll
        for (int mt = 0; mt < 4; ++mt) xoff[mt] = lbase + (8 * g + q4) * 256 + (((4 * rg + mt) ^ fl) * 32) + (lane & 3) * 8;
        const u32 woff = lbase + 16384 + hh * 3 * 1024 + lane * 16;
        struct Frag { u32x2 lo[4], hi[4]; u32x4 wq, wk, wv; };
        auto rd = [&](Frag& f, int kc, int ks) {
            const u32 so = (kc % QNS) * QSLOT;
#pragma unroll
            for (int mt = 0; mt < 4; ++mt) {
                u32 a = xoff[mt] + so;
                if (ks == 0) {
                    asm volatile("ds_read_b64_tr_b16 %0, %1" : "=v"(f.lo[mt]) : "v"(a));
                    asm volatile("ds_read_b64_tr_b16 %0, %1 offset:1024" : "=v"(f.hi[mt]) : "v"(a));
                } else {
                    asm volatile("ds_read_b64_tr_b16 %0, %1 offset:8192" : "=v"(f.lo[mt]) : "v"(a));
                    asm volatile("ds_read_b64_tr_b16 %0, %1 offset:9216" : "=v"(f.hi[mt]) : "v"(a));
                }
            }
            u32 a = woff + so;
            if (ks == 0) {
                asm volatile("ds_read_b128 %0, %1" : "=v"(f.wq) : "v"(a));
                asm volatile("ds_read_b128 %0, %1 offset:1024" : "=v"(f.wk) : "v"(a));
                asm volatile("ds_read_b128 %0, %1 offset:2048" : "=v"(f.wv) : "v"(a));
            } else {
                asm volatile("ds_read_b128 %0, %1 offset:6144" : "=v"(f.wq) : "v"(a));
                asm volatile("ds_read_b128 %0, %1 offset:7168" : "=v"(f.wk) : "v"(a));
                asm volatile("ds_read_b128 %0, %1 offset:8192" : "=v"(f.wv) : "v"(a));
            }
        };
#define Q_WAIT(F, N) asm volatile("s_waitcnt lgkmcnt(" #N ")" : "+v"(F.lo[0]), "+v"(F.hi[0]), "+v"(F.lo[1]), "+v"(F.hi[1]), \
        "+v"(F.lo[2]), "+v"(F.hi[2]), "+v"(F.lo[3]), "+v"(F.hi[3]), "+v"(F.wq), "+v"(F.wk), "+v"(F.wv) :: "memory")
        auto mm = [&](const Frag& f) {
            bf16x8 wq = __builtin_bit_cast(bf16x8, f.wq), wk = __builtin_bit_cast(bf16x8, f.wk), wv = __builtin_bit_cast(bf16x8, f.wv);
            bf16x8 xa[4];
#pragma unroll
            for (int mt = 0; mt < 4; ++mt) xa[mt] = __builtin_bit_cast(bf16x8, u32x4{f.lo[mt][0], f.lo[mt][1], f.hi[mt][0], f.hi[mt][1]});
#pragma unroll
            for (int mt = 0; mt < 4; ++mt) {
                aq[mt] = mfma_bf(wq, xa[mt], aq[mt]);
                ak[mt] = mfma_bf(xa[mt], wk, ak[mt]);
                av[mt] = mfma_bf(xa[mt], wv, av[mt]);
            }
            if (hh) {
#pragma unroll
                for (int i = 0; i < 2; ++i) { as[i] = mfma_bf(xa[2 + i], xa[2 + i], as[i]); as[2 + i] = mfma_bf(xa[2 + i], onesv, as[2 + i]); }
            } else {
#pragma unroll
                for (int i = 0; i < 2; ++i) { as[i] = mfma_bf(xa[i], xa[i], as[i]); as[2 + i] = mfma_bf(xa[i], onesv, as[2 + i]); }
            }
        };
        Frag fa, fb;
        asm volatile("s_waitcnt vmcnt(0) lgkmcnt(0)" ::: "memory");
        __builtin_amdgcn_s_barrier();
        asm volatile("" ::: "memory");
        STAMP(1);
        rd(fa, 0, 0);
#pragma unroll
        for (int kc = 0; kc < 8; ++kc) {
            rd(fb, kc, 1);
            Q_WAIT(fa, 11);
            mm(fa);
            Q_WAIT(fb, 0);
            if (kc < 7) {
                __builtin_amdgcn_s_barrier();
                asm volatile("" ::: "memory");
                STAMP(3 + kc);
                rd(fa, kc + 1, 0);
            }
            mm(fb);
        }
#undef Q_WAIT
        STAMP(10);
#pragma unroll
        for (int i = 0; i < 2; ++i) {
            *(f32x4*)(hand + ((w * 6 + 3 * i) * 64 + lane) * 16) = aq[2 + i];
            *(f32x4*)(hand + ((w * 6 + 3 * i + 1) * 64 + lane) * 16) = ak[2 + i];
            *(f32x4*)(hand + ((w * 6 + 3 * i + 2) * 64 + lane) * 16) = av[2 + i];
        }
#pragma unroll
        for (int i = 0; i < 2; ++i) {
            const int rgi = j & 3;
            const float sq = rgi == 0 ? as[i][0] : (rgi == 1 ? as[i][1] : (rgi == 2 ? as[i][2] : as[i][3]));
            const float meanj = (rgi == 0 ? as[2 + i][0] : (rgi == 1 ? as[2 + i][1] : (rgi == 2 ? as[2 + i][2] : as[2 + i][3]))) * (1.0f / DM);
            float mb, rs;
            ln_scalars(meanj, fmaxf(fmaf(-meanj, meanj, sq * (1.0f / DM)), 0.f), mb, rs);
            if (g == (j >> 2)) { lnm[rg * 64 + (2 * hh + i) * 16 + j] = mb; lnr[rg * 64 + (2 * hh + i) * 16 + j] = rs; }
        }
    }
    lds_barrier();
    if (w >= 4) {
#pragma unroll
        for (int i = 0; i < 2; ++i) {
            aq[i] = *(const f32x4*)(hand + (((w - 4) * 6 + 3 * i) * 64 + lane) * 16);
            ak[i] = *(const f32x4*)(hand + (((w - 4) * 6 + 3 * i + 1) * 64 + lane) * 16);
            av[i] = *(const f32x4*)(hand + (((w - 4) * 6 + 3 * i + 2) * 64 + lane) * 16);
        }
    }
    STAMP(11);
    const int mt0 = (w >> 2) * 2;
    const f32x4 wsq = *(const f32x4*)(wst + hh * 48 + 4 * g);
    const float wsk = wst[hh * 48 + 16 + j], wsv = wst[hh * 48 + 32 + j];
    u32 pk8[4], vv8[4];
#pragma unroll
    for (int i = 0; i < 2; ++i) {
        const int r0 = rg * 64 + (mt0 + i) * 16;
        const float m1 = lnm[r0 + j], r1 = lnr[r0 + j];
        const u32 q01 = phi2((f32x2{aq[i][0], aq[i][1]} - f32x2{wsq[0], wsq[1]} * m1) * r1);
        const u32 q23 = phi2((f32x2{aq[i][2], aq[i][3]} - f32x2{wsq[2], wsq[3]} * m1) * r1);
        const int lt = rt * 8 + rg * 4 + mt0 + i;
        st8(pqF + ((size_t)(lt * 16 + hp) * 64 + lane) * 8 + hh * 4, u32x2{q01, q23});
        const f32x4 mr = *(const f32x4*)(lnm + r0 + 4 * g), rr = *(const f32x4*)(lnr + r0 + 4 * g);
        const f32x2 m01 = {mr[0], mr[1]}, m23 = {mr[2], mr[3]}, r01 = {rr[0], rr[1]}, r23 = {rr[2], rr[3]};
        pk8[2 * i] = phi2((f32x2{ak[i][0], ak[i][1]} - m01 * wsk) * r01);
        pk8[2 * i + 1] = phi2((f32x2{ak[i][2], ak[i][3]} - m23 * wsk) * r23);
        const f32x2 v01 = (f32x2{av[i][0], av[i][1]} - m01 * wsv) * r01, v23 = (f32x2{av[i][2], av[i][3]} - m23 * wsv) * r23;
        vv8[2 * i] = pk_bf(v01[0], v01[1]); vv8[2 * i + 1] = pk_bf(v23[0], v23[1]);
    }
    const u32 ones = 0x3f803f80u;
    const bf16x8 pk = __builtin_bit_cast(bf16x8, u32x4{pk8[0], pk8[1], pk8[2], pk8[3]});
    const bf16x8 vv = __builtin_bit_cast(bf16x8, u32x4{vv8[0], vv8[1], vv8[2], vv8[3]});
    const f32x4 z4 = {0.f, 0.f, 0.f, 0.f};
    const f32x4 kvT = mfma_bf(vv, pk, z4);
    const f32x4 zz = mfma_bf(__builtin_bit_cast(bf16x8, u32x4{ones, ones, ones, ones}), pk, z4);
    STAMP(12);
    float* red = (float*)(ring + QSLOT);
    float* rp = red + (hh * 4 + (w >> 1)) * KVH;
#pragma unroll
    for (int e = 0; e < 4; ++e) rp[(4 * g + e) * 16 + j] = kvT[e];
    if (g == 0) rp[256 + j] = zz[0];
    lds_barrier();
    for (int i = t; i < 2 * KVH; i += 512) {
        int h2 = i >= KVH, ii = i - h2 * KVH;
        const float* rq = red + h2 * 4 * KVH + ii;
        unsafeAtomicAdd(kvacc + (2 * hp + h2) * KVH + ii, (rq[0] + rq[KVH]) + (rq[2 * KVH] + rq[3 * KVH]));
    }
    STAMP_END();
}

template <int FINAL>
__global__ __launch_bounds__(512, 1) void oproj_kernel(
    const u16* __restrict__ xT, const u16* __restrict__ pqF, const float* __restrict__ kvacc,
    const u16* __restrict__ woF, u16* __restrict__ xo, f32x2* __restrict__ stats DBG_ARG) {
    STAMP(0);
    __shared__ __attribute__((aligned(16))) char sm[32768 + 128 * 72 + 32 * 272 + 2048];
    char* OF = sm; char* XT = sm + 32768; char* OT = XT + 128 * 72; f32x2* ST = (f32x2*)(OT + 32 * 272);
    const int t = threadIdx.x, lane = t & 63, w = t >> 6, g = lane >> 4, j = lane & 15;
    const int xcd = blockIdx.x & 7, jb = blockIdx.x >> 3;
    const int rt = xcd * 8 + (jb >> 2), cq = jb & 3, l0 = rt * 32;
    u32x4 pq[2][2];
#pragma unroll
    for (int lt = 0; lt < 2; ++lt)
#pragma unroll
        for (int i = 0; i < 2; ++i) pq[lt][i] = *(const u32x4*)(pqF + ((size_t)((2 * rt + lt) * 16 + 2 * w + i) * 64 + lane) * 8);
    f32x4 kf[4], zf[4];
#pragma unroll
    for (int i = 0; i < 4; ++i) {
        kf[i] = *(const f32x4*)(kvacc + (4 * w + i) * KVH + j * 16 + 4 * g);
        zf[i] = *(const f32x4*)(kvacc + (4 * w + i) * KVH + 256 + 4 * g);
    }
    u32x4 xin = *(const u32x4*)(xT + (size_t)(cq * 128 + (t >> 2)) * XP + l0 + (t & 3) * 8);
    bf16x8 wf[16];
#pragma unroll
    for (int ks = 0; ks < 16; ++ks) wf[ks] = *(const bf16x8*)(woF + ((size_t)((cq * 8 + w) * 16 + ks) * 64 + lane) * 8);
    {
        char* p = XT + (t >> 2) * 72 + (t & 3) * 16;
        *(u32x2*)p = u32x2{xin[0], xin[1]}; *(u32x2*)(p + 8) = u32x2{xin[2], xin[3]};
    }
#pragma unroll
    for (int i = 0; i < 2; ++i) {
        u32x4 of[2];
#pragma unroll
        for (int h2 = 0; h2 < 2; ++h2) {
            f32x4 k4 = kf[2 * i + h2], z4 = zf[2 * i + h2];
            s16x4 ka = __builtin_bit_cast(s16x4, u32x2{pk_bf(k4[0], k4[1]), pk_bf(k4[2], k4[3])});
            s16x4 za = __builtin_bit_cast(s16x4, u32x2{pk_bf(z4[0], z4[1]), pk_bf(z4[2], z4[3])});
#pragma unroll
            for (int lt = 0; lt < 2; ++lt) {
                s16x4 b = __builtin_bit_cast(s16x4, u32x2{pq[lt][i][2 * h2], pq[lt][i][2 * h2 + 1]});
                f32x4 zero = {0.f, 0.f, 0.f, 0.f};
                f32x4 num = mfma_bf16k(ka, b, zero), den = mfma_bf16k(za, b, zero);
                float rd = __builtin_amdgcn_rcpf(rbf(rbf(den[0]) + 1e-6f));
                const u32 n01 = pk_bf(num[0], num[1]), n23 = pk_bf(num[2], num[3]);
                const f32x2 o01 = f32x2{bf_lo(n01), bf_hi(n01)} * rd, o23 = f32x2{bf_lo(n23), bf_hi(n23)} * rd;
                of[lt][2 * h2] = pk_bf(o01[0], o01[1]);
                of[lt][2 * h2 + 1] = pk_bf(o23[0], o23[1]);
            }
        }
#pragma unroll
        for (int lt = 0; lt < 2; ++lt) *(u32x4*)(OF + (lt * 16 + 2 * w + i) * 1024 + lane * 16) = of[lt];
    }
    STAMP(1);
    lds_barrier();
    STAMP(2);
    f32x4 acc[2] = {{0.f, 0.f, 0.f, 0.f}, {0.f, 0.f, 0.f, 0.f}};
#pragma unroll
    for (int ks = 0; ks < 16; ++ks)
#pragma unroll
        for (int lt = 0; lt < 2; ++lt)
            acc[lt] = mfma_bf(wf[ks], *(const bf16x8*)(OF + (lt * 16 + ks) * 1024 + lane * 16), acc[lt]);
    STAMP(3);
#pragma unroll
    for (int lt = 0; lt < 2; ++lt) {
        float r[4];
#pragma unroll
        for (int e = 0; e < 4; ++e) {
            u16* px = (u16*)(XT + (16 * w + 4 * g + e) * 72) + 16 * lt + j;
            r[e] = rbf(bf2f(*px) + rbf(acc[lt][e]));
            if (!FINAL) *px = (u16)(__float_as_uint(r[e]) >> 16);
        }
        if (FINAL) {
            *(u32x2*)(OT + (16 * lt + j) * 272 + (16 * w + 4 * g) * 2) = u32x2{pk_bf(r[0], r[1]), pk_bf(r[2], r[3])};
        } else {
            float s = (r[0] + r[1]) + (r[2] + r[3]);
            s = quad_rows_sum(s);
            float mean = s * (1.0f / 16.0f), q = 0.f;
#pragma unroll
            for (int e = 0; e < 4; ++e) { float d = r[e] - mean; q = fmaf(d, d, q); }
            q = quad_rows_sum(q);
            if (g == 0) ST[w * 32 + 16 * lt + j] = f32x2{mean, q};
        }
    }
    lds_barrier();
    if (FINAL) {
        u32x4 o = *(const u32x4*)(OT + (t >> 4) * 272 + (t & 15) * 16);
        st16(xo + (size_t)(l0 + (t >> 4)) * DM + cq * 128 + (t & 15) * 8, o);
    } else {
        const char* p = XT + (t >> 2) * 72 + (t & 3) * 16;
        u32x2 a = *(const u32x2*)p, c = *(const u32x2*)(p + 8);
        st16(xo + (size_t)(cq * 128 + (t >> 2)) * XP + l0 + (t & 3) * 8, u32x4{a[0], a[1], c[0], c[1]});
        if (t < 32) {
            float mean = 0.f, m2 = 0.f;
#pragma unroll
            for (int i = 0; i < 8; ++i) { f32x2 s = ST[i * 32 + t]; mean += s[0]; m2 += s[1]; }
            mean *= 0.125f;
#pragma unroll
            for (int i = 0; i < 8; ++i) { float d = ST[i * 32 + t][0] - mean; m2 = fmaf(16.0f * d, d, m2); }
            stats[cq * SL + l0 + t] = f32x2{mean, m2};
        }
    }
    STAMP_END();
}

extern "C" void kernel_launch(void* const* d_in, const int* in_sizes, int n_in,
                              void* d_out, int out_size, void* d_ws, size_t ws_size,
                              hipStream_t stream) {
    const u16* u = (const u16*)d_in[0];
    const u16* lar = (const u16*)d_in[1];
    const u16* aim = (const u16*)d_in[2];
    const u16* cre = (const u16*)d_in[3];
    const u16* cim = (const u16*)d_in[4];
    const u16* ldt = (const u16*)d_in[5];
    const u16* dsk = (const u16*)d_in[6];
    const u16* wqkv = (const u16*)d_in[8];
    const u16* wo = (const u16*)d_in[9];
    char* ws = (char*)d_ws;
    const size_t MB = 1 << 20;
    u16* xT0 = (u16*)(ws + 18 * MB);
    u16* xT1 = (u16*)(ws + 21 * MB);
    u16* xT2 = (u16*)(ws + 24 * MB);
    u16* xT3 = (u16*)(ws + 27 * MB);
    u16* pqF = (u16*)(ws + 8 * MB);
    u16* Kr = (u16*)(ws + 10 * MB);
    u16* wqF = (u16*)(ws + 13 * MB);
    u16* woF = (u16*)(ws + 15 * MB);
    f32x2* st0 = (f32x2*)(ws + 16 * MB);
    f32x2* st2 = (f32x2*)(ws + 16 * MB + 65536);
    float* Kscale = (float*)(ws + 16 * MB + 196608);
    float* Wsum = (float*)(ws + 16 * MB + 200704);
    float* kvacc = (float*)(ws + 16 * MB + 262144);
    prep_kernel<<<256, 768, 0, stream>>>(u, lar, aim, cre, cim, ldt, wqkv, wo, xT0, st0, Kr, Kscale,
                                                               wqF, Wsum, woF, kvacc DBG_PASS(0));
    conv_kernel<1><<<256, 512, 0, stream>>>(xT0, st0, Kr, Kscale, dsk, xT1 DBG_PASS(1));
    qkv_kernel<<<256, 512, 0, stream>>>(xT1, wqF, Wsum, pqF, kvacc DBG_PASS(2));
    oproj_kernel<0><<<256, 512, 0, stream>>>(xT1, pqF, kvacc, woF, xT2, st2 DBG_PASS(3));
    conv_kernel<4><<<256, 512, 0, stream>>>(xT2, st2, Kr, Kscale, dsk, xT3 DBG_PASS(4));
    qkv_kernel<<<256, 512, 0, stream>>>(xT3, wqF, Wsum, pqF, kvacc + 32 * KVH DBG_PASS(5));
    oproj_kernel<1><<<256, 512, 0, stream>>>(xT3, pqF, kvacc + 32 * KVH, woF, (u16*)d_out, st2 DBG_PASS(6));
}
```

```cpp
#include <hip/hip_runtime.h>
#include <hip/hip_bf16.h>
#include <stdint.h>

typedef unsigned short u16;
typedef unsigned int u32;
typedef __attribute__((ext_vector_type(4))) float f32x4;
typedef __attribute__((ext_vector_type(2))) float f32x2;
typedef __attribute__((ext_vector_type(8))) __bf16 bf16x8;
typedef __attribute__((ext_vector_type(2))) __bf16 bf16x2;
typedef __attribute__((ext_vector_type(8))) _Float16 f16x8;
typedef __attribute__((ext_vector_type(2))) _Float16 f16x2;
typedef __attribute__((ext_vector_type(4))) short s16x4;
typedef __attribute__((ext_vector_type(8))) short s16x8;
typedef __attribute__((ext_vector_type(4))) u32 u32x4;
typedef __attribute__((ext_vector_type(2))) u32 u32x2;

#define DEVI __device__ __forceinline__
#define DBG_ARG
#define DBG_PASS(k)
#define STAMP(i)
#define STAMP_END()

constexpr int SL = 2048;
constexpr int DM = 512;
constexpr int NST = 128;
constexpr int XP = 2176;
constexpr int KRL = 2056;
constexpr int KVH = 272;

DEVI float bf_lo(u32 v) { return __uint_as_float(v << 16); }
DEVI float bf_hi(u32 v) { return __uint_as_float(v & 0xffff0000u); }
DEVI float bf2f(u16 v) { return __uint_as_float(((u32)v) << 16); }
DEVI u32 pk_bf(float a, float b) {
    f32x2 x = {a, b};
    return __builtin_bit_cast(u32, __builtin_convertvector(x, bf16x2));
}
DEVI u32 pk_h(float a, float b) {
    f32x2 x = {a, b};
    return __builtin_bit_cast(u32, __builtin_convertvector(x, f16x2));
}
DEVI float rbf(float a) { return bf_lo(pk_bf(a, 0.f)); }
template <int C> DEVI float dpp_f(float x) {
    return __int_as_float(__builtin_amdgcn_update_dpp(0, __float_as_int(x), C, 0xf, 0xf, true));
}
DEVI float row16_sum(float x) {
    x += dpp_f<0xB1>(x); x += dpp_f<0x4E>(x); x += dpp_f<0x141>(x); x += dpp_f<0x140>(x);
    return x;
}
DEVI float quad_rows_sum(float x) {
    u32x2 a = __builtin_amdgcn_permlane16_swap(__float_as_uint(x), __float_as_uint(x), false, false);
    x = __uint_as_float(a[0]) + __uint_as_float(a[1]);
    u32x2 b = __builtin_amdgcn_permlane32_swap(__float_as_uint(x), __float_as_uint(x), false, false);
    return __uint_as_float(b[0]) + __uint_as_float(b[1]);
}
DEVI float rdl(float x, int l) { return __int_as_float(__builtin_amdgcn_readlane(__float_as_int(x), l)); }
DEVI float wave_sum(float x) {
    x = row16_sum(x);
    return (rdl(x, 0) + rdl(x, 16)) + (rdl(x, 32) + rdl(x, 48));
}
DEVI float wave_max(float x) {
    x = fmaxf(x, dpp_f<0xB1>(x)); x = fmaxf(x, dpp_f<0x4E>(x));
    x = fmaxf(x, dpp_f<0x141>(x)); x = fmaxf(x, dpp_f<0x140>(x));
    return fmaxf(fmaxf(rdl(x, 0), rdl(x, 16)), fmaxf(rdl(x, 32), rdl(x, 48)));
}
DEVI f32x4 mfma_bf(bf16x8 a, bf16x8 b, f32x4 c) { return __builtin_amdgcn_mfma_f32_16x16x32_bf16(a, b, c, 0, 0, 0); }
DEVI f32x4 mfma_h(f16x8 a, f16x8 b, f32x4 c) { return __builtin_amdgcn_mfma_f32_16x16x32_f16(a, b, c, 0, 0, 0); }
DEVI f32x4 mfma_bf16k(s16x4 a, s16x4 b, f32x4 c) { return __builtin_amdgcn_mfma_f32_16x16x16bf16_1k(a, b, c, 0, 0, 0); }
#define WT 1
DEVI void st16(void* p, u32x4 v) {
    asm volatile("global_store_dwordx4 %0, %1, off sc1\n\ts_nop 1" ::"v"(p), "v"(v) : "memory");
}
DEVI void st8(void* p, u32x2 v) {
    __hip_atomic_store((unsigned long long*)p, __builtin_bit_cast(unsigned long long, v), __ATOMIC_RELAXED, __HIP_MEMORY_SCOPE_AGENT);
}
DEVI void lds_barrier() {
    asm volatile("s_waitcnt lgkmcnt(0)" ::: "memory");
    __builtin_amdgcn_s_barrier();
    asm volatile("" ::: "memory");
}
DEVI void ln_scalars(float mean, float var, float& mb, float& rs) {
    mb = rbf(mean);
    float sb = rbf(__builtin_amdgcn_sqrtf(rbf(rbf(var) + 1e-5f)));
    rs = __builtin_amdgcn_rcpf(sb);
}

constexpr int P_TR = 256, P_KG = 512;

__global__ __launch_bounds__(256, 2) void prep_kernel(
    const u16* __restrict__ x, const u16* __restrict__ lar, const u16* __restrict__ aim,
    const u16* __restrict__ cre, const u16* __restrict__ cim, const u16* __restrict__ ldt,
    const u16* __restrict__ wqkv, const u16* __restrict__ wo,
    u16* __restrict__ xT, f32x2* __restrict__ stats, u16* __restrict__ Kr, float* __restrict__ Kscale,
    u16* __restrict__ wqF, float* __restrict__ Wsum, u16* __restrict__ woF, float* __restrict__ kvacc DBG_ARG) {
    STAMP(0);
    __shared__ __attribute__((aligned(16))) char sm[24576];
    const int t = threadIdx.x, lane = t & 63, w = __builtin_amdgcn_readfirstlane(t >> 6), g = lane >> 4, j = lane & 15;
    int b = blockIdx.x;
    if (b < P_TR) {
        const int r0 = ((b & 7) * 32 + (b >> 3)) * 8;
        u32 v[8];
        float pv[8];
#pragma unroll
        for (int i = 0; i < 8; ++i) {
            v[i] = *(const u32*)(x + (size_t)(r0 + i) * DM + 2 * t);
            pv[i] = bf2f(x[(size_t)(r0 + i) * DM]);
        }
        u16* dst = xT + (size_t)(2 * t) * XP + r0;
        *(u32x4*)dst = u32x4{__builtin_amdgcn_perm(v[1], v[0], 0x05040100), __builtin_amdgcn_perm(v[3], v[2], 0x05040100),
                             __builtin_amdgcn_perm(v[5], v[4], 0x05040100), __builtin_amdgcn_perm(v[7], v[6], 0x05040100)};
        *(u32x4*)(dst + XP) = u32x4{__builtin_amdgcn_perm(v[1], v[0], 0x07060302), __builtin_amdgcn_perm(v[3], v[2], 0x07060302),
                                    __builtin_amdgcn_perm(v[5], v[4], 0x07060302), __builtin_amdgcn_perm(v[7], v[6], 0x07060302)};
        float u8[8];
#pragma unroll
        for (int i = 0; i < 8; ++i) {
            const float d0 = bf_lo(v[i]) - pv[i], d1 = bf_hi(v[i]) - pv[i];
            u32x2 sw = __builtin_amdgcn_permlane32_swap(__float_as_uint(d0 + d1), __float_as_uint(fmaf(d0, d0, d1 * d1)), false, false);
            u8[i] = __uint_as_float(sw[0]) + __uint_as_float(sw[1]);
        }
        float* red = (float*)sm;
#pragma unroll
        for (int i = 0; i < 4; ++i) {
            u32x2 sw = __builtin_amdgcn_permlane16_swap(__float_as_uint(u8[i]), __float_as_uint(u8[i + 4]), false, false);
            const float tot = row16_sum(__uint_as_float(sw[0]) + __uint_as_float(sw[1]));
            if (j == 0) red[w * 16 + g * 4 + i] = tot;
        }
        lds_barrier();
        if (t < 8) {
            const float S = (red[t] + red[16 + t]) + (red[32 + t] + red[48 + t]);
            const float Q = (red[8 + t] + red[24 + t]) + (red[40 + t] + red[56 + t]);
            const float p = bf2f(x[(size_t)(r0 + t) * DM]);
            stats[r0 + t] = f32x2{fmaf(S, 1.0f / DM, p), fmaxf(fmaf(-S * (1.0f / DM), S, Q), 0.f)};
        }
        STAMP_END();
        return;
    }
    b -= P_TR;
    {
        const int d = b;
        const u16* pc = cre + d * NST; const u16* pi = cim + d * NST;
        float cm = fmaxf(fabsf(bf2f(pc[lane])) + fabsf(bf2f(pi[lane])),
                         fabsf(bf2f(pc[lane + 64])) + fabsf(bf2f(pi[lane + 64])));
        const int ns = d * NST + 32 * w + (lane & 31);
        const u16 pl = lar[ns], pa = aim[ns], pcr = cre[ns], pci = cim[ns];
        const float dt = rbf(__expf(bf2f(ldt[d])));
        cm = wave_max(cm);
        const float cinv = cm > 0.f ? 1.0f / cm : 0.f;
        float* spar = (float*)(sm + 22528) + w * 128;
        if (lane < 32) {
            spar[lane] = -1.4426950408889634f * dt * rbf(__expf(bf2f(pl)));
            spar[32 + lane] = 0.15915494309189535f * dt * bf2f(pa);
            spar[64 + lane] = bf2f(pcr) * cinv;
            spar[96 + lane] = bf2f(pci) * cinv;
        }
        STAMP(2);
        __builtin_amdgcn_sched_barrier(0);
        u16 we[8];
        const bool isq = b < 384;
        const int wnt = (b >> 2) % 6, whp = (b >> 2) / 6;
        const int wcol = (wnt % 3) * DM + (2 * whp + wnt / 3) * 16 + j;
        const int wks = 4 * (b & 3) + w, wct = (b - 384) >> 2, wh2 = 4 * w + (b & 3);
        const char* wbase = (const char*)(isq ? wqkv : wo);
        const u32 woff = 2u * (isq ? (32 * wks + 8 * g) * (3 * DM) + wcol : (32 * wh2 + 4 * g) * DM + wct * 16 + j);
        u16* wdst = (isq ? wqF : woF) + 8u * (u32)((isq ? (whp * 16 + wks) * 6 + wnt : wct * 16 + wh2) * 64 + lane);
#pragma unroll
        for (int jj = 0; jj < 8; ++jj)
            we[jj] = *(const u16*)(wbase + (woff + (u32)(isq ? jj * (6 * DM) : ((jj >> 2) * 16 + (jj & 3)) * (2 * DM))));
        if (!isq && t < 136) kvacc[(b - 384) * 136 + t] = 0.f;
        __builtin_amdgcn_sched_barrier(0);
        f32x4 acc[4];
#pragma unroll
        for (int s = 0; s < 4; ++s) acc[s] = f32x4{0.f, 0.f, 0.f, 0.f};
        const float fj = (float)j;
#pragma unroll
        for (int ts = 0; ts < 2; ++ts) {
            u32x4 fr[8];
            f32x4 pq4[4];
#pragma unroll
            for (int i = 0; i < 4; ++i) pq4[i] = *(const f32x4*)(spar + 32 * i + 16 * ts + 4 * g);
#pragma unroll
            for (int e2 = 0; e2 < 2; ++e2) {
                const f32x2 ra = {pq4[0][2 * e2], pq4[0][2 * e2 + 1]}, rb = {pq4[1][2 * e2], pq4[1][2 * e2 + 1]};
                const f32x2 fc = {pq4[2][2 * e2], pq4[2][2 * e2 + 1]}, fi = {pq4[3][2 * e2], pq4[3][2 * e2 + 1]};
                const f32x2 nfc = -fc;
                const f32x2 me = ra * fj;
                f32x2 rr = rb * fj;
                rr -= f32x2{rintf(rr[0]), rintf(rr[1])};
                const f32x2 mm = {__builtin_amdgcn_exp2f(me[0]), __builtin_amdgcn_exp2f(me[1])};
                f32x2 pr = mm * f32x2{__builtin_amdgcn_cosf(rr[0]), __builtin_amdgcn_cosf(rr[1])};
                f32x2 pi_ = mm * f32x2{__builtin_amdgcn_sinf(rr[0]), __builtin_amdgcn_sinf(rr[1])};
#pragma unroll
                for (int k = 0; k < 8; ++k) {
                    if (k > 0) {
                        f32x2 nr = pr * pr - pi_ * pi_, ni = (pr + pr) * pi_;
                        pr = nr; pi_ = ni;
                    }
                    f32x2 qr = pr, qi = pi_;
                    if (k == 7) {
                        f32x2 sr = {dpp_f<0x108>(pr[0]), dpp_f<0x108>(pr[1])}, si = {dpp_f<0x108>(pi_[0]), dpp_f<0x108>(pi_[1])};
                        f32x2 tr = sr * sr - si * si, ti = (sr + sr) * si;
                        if (j == 0) { qr = tr; qi = ti; }
                    }
                    if (k < 4) { fr[k][2 * e2] = pk_h(qr[0], qi[0]); fr[k][2 * e2 + 1] = pk_h(qr[1], qi[1]); }
                    else {
                        f32x2 br = fc * qr - fi * qi, bi = nfc * qi - fi * qr;
                        fr[k][2 * e2] = pk_h(br[0], bi[0]); fr[k][2 * e2 + 1] = pk_h(br[1], bi[1]);
                    }
                }
            }
#pragma unroll
            for (int s = 0; s < 4; ++s)
                acc[s] = mfma_h(__builtin_bit_cast(f16x8, fr[s]), __builtin_bit_cast(f16x8, fr[4 + s]), acc[s]);
            if (ts == 0) STAMP(3);
        }
        STAMP(1);
        __builtin_amdgcn_sched_barrier(0);
        float* part = (float*)sm;
        float* tab = part + 4096;
        float* wred = tab + 1280;
#pragma unroll
        for (int s = 0; s < 4; ++s) *(f32x4*)(part + (w * 4 + s) * 256 + j * 16 + 4 * g) = acc[s];
        {
            u32x4 o;
            float sw = 0.f;
#pragma unroll
            for (int jj = 0; jj < 4; ++jj) {
                o[jj] = (u32)we[2 * jj] | ((u32)we[2 * jj + 1] << 16);
                sw += bf2f(we[2 * jj]) + bf2f(we[2 * jj + 1]);
            }
            wred[t] = sw;
            lds_barrier();
            STAMP(4);
            st16(wdst, o);
            if (isq && t < 16) {
                float a = 0.f;
#pragma unroll
                for (int i = 0; i < 16; ++i) a += wred[i * 16 + t];
                Wsum[(b & 3) * (3 * DM) + wcol] = a;
            }
        }
#pragma unroll
        for (int s = 0; s < 4; ++s) {
            const float v = (part[s * 256 + t] + part[(4 + s) * 256 + t]) + (part[(8 + s) * 256 + t] + part[(12 + s) * 256 + t]);
            tab[s * 256 + t] = v;
            if (s == 3 && t == 0) tab[1152] = v;
        }
        lds_barrier();
        float kv[8];
        {
            const int lb = 2040 - 8 * t;
            const bool big = lb >= 128;
#pragma unroll
            for (int e = 0; e < 8; e += 2) {
                const int l = lb + 7 - e;
                const u32 pb = pk_bf((float)l, (float)(l - 1));
                const u32 h0 = pb & 0xffffu, h1 = pb >> 16;
                kv[e] = tab[big ? (int)(h0 + (h0 & 0x7f80u)) - 34176 : l];
                kv[e + 1] = tab[big ? (int)(h1 + (h1 & 0x7f80u)) - 34176 : l - 1];
            }
        }
        u16* dst = Kr + (size_t)d * KRL;
        st16(dst + 8 * t, u32x4{pk_h(kv[0], kv[1]), pk_h(kv[2], kv[3]), pk_h(kv[4], kv[5]), pk_h(kv[6], kv[7])});
        if (t == 0) { *(u32x4*)(dst + 2048) = u32x4{0, 0, 0, 0}; Kscale[d] = 2.0f * dt * cm; }
        STAMP_END();
    }
}

constexpr int RCP = 4160;
constexpr int UP = (256 + SL) * 2;
template <int NCT>
__global__ __launch_bounds__(512, 1) void conv_kernel(
    const u16* __restrict__ xT, const f32x2* __restrict__ stats, const u16* __restrict__ Kr,
    const float* __restrict__ Kscale, const u16* __restrict__ dskip, u16* __restrict__ xo DBG_ARG) {
    STAMP(0);
    __shared__ __attribute__((aligned(16))) char sm[8 * RCP + 2 * UP + 16384 + 32768];
    char* RC = sm; char* U = sm + 8 * RCP; char* XU = U + 2 * UP; char* RED = XU + 16384;
    const int t = threadIdx.x, lane = t & 63, g = lane >> 4, r = lane & 15;
    const int w = __builtin_amdgcn_readfirstlane(t >> 6);
    const int c0 = 2 * blockIdx.x;
    u32x2 xv[2];
#pragma unroll
    for (int ch = 0; ch < 2; ++ch) xv[ch] = *(const u32x2*)(xT + (size_t)(c0 + ch) * XP + 4 * t);
    const int kch = t >> 8, kt = t & 255;
    const u32x4 ka = *(const u32x4*)(Kr + (size_t)(c0 + kch) * KRL + 8 * kt);
    const u32x2 kb = *(const u32x2*)(Kr + (size_t)(c0 + kch) * KRL + 8 * kt + 8);
    f32x4 sv[NCT][2];
#pragma unroll
    for (int ct = 0; ct < NCT; ++ct)
#pragma unroll
        for (int i = 0; i < 2; ++i) sv[ct][i] = *(const f32x4*)(stats + ct * SL + 4 * t + 2 * i);
    const float ds[2] = {bf2f(dskip[c0]), bf2f(dskip[c0 + 1])};
    const float ksc = Kscale[c0 + (w >> 2)];
    if (t < 64) *(u32x4*)(U + (t >> 5) * UP + (t & 31) * 16) = u32x4{0, 0, 0, 0};
    else if (t < 80) *(u32x4*)(RC + ((t - 64) >> 1) * RCP + 4096 + (t & 1) * 16) = u32x4{0, 0, 0, 0};
    {
        u32 d0 = ka[0], d1 = ka[1], d2 = ka[2], d3 = ka[3], d4 = kb[0], d5 = kb[1];
        u32 a0 = __builtin_amdgcn_alignbit(d1, d0, 16), a1 = __builtin_amdgcn_alignbit(d2, d1, 16);
        u32 a2 = __builtin_amdgcn_alignbit(d3, d2, 16), a3 = __builtin_amdgcn_alignbit(d4, d3, 16);
        u32 a4 = __builtin_amdgcn_alignbit(d5, d4, 16);
        char* p = RC + kch * 4 * RCP + kt * 16;
        *(u32x4*)(p) = u32x4{d0, d1, d2, d3};
        *(u32x4*)(p + RCP) = u32x4{a0, a1, a2, a3};
        *(u32x4*)(p + 2 * RCP) = u32x4{d1, d2, d3, d4};
        *(u32x4*)(p + 3 * RCP) = u32x4{a1, a2, a3, a4};
    }
    STAMP(1);
    float mb[4], rs[4];
#pragma unroll
    for (int e = 0; e < 4; ++e) {
        float mean, m2;
        if (NCT == 1) { mean = sv[0][e >> 1][(e & 1) * 2]; m2 = sv[0][e >> 1][(e & 1) * 2 + 1]; }
        else {
            mean = 0.f; m2 = 0.f;
#pragma unroll
            for (int ct = 0; ct < NCT; ++ct) { mean += sv[ct][e >> 1][(e & 1) * 2]; m2 += sv[ct][e >> 1][(e & 1) * 2 + 1]; }
            mean *= (1.0f / NCT);
#pragma unroll
            for (int ct = 0; ct < NCT; ++ct) { float dd = sv[ct][e >> 1][(e & 1) * 2] - mean; m2 = fmaf(dd * (float)(DM / NCT), dd, m2); }
        }
        ln_scalars(mean, m2 * (1.0f / DM), mb[e], rs[e]);
    }
#pragma unroll
    for (int ch = 0; ch < 2; ++ch) {
        float xf[4] = {bf_lo(xv[ch][0]), bf_hi(xv[ch][0]), bf_lo(xv[ch][1]), bf_hi(xv[ch][1])};
        u32 d01 = pk_bf(xf[0] - mb[0], xf[1] - mb[1]), d23 = pk_bf(xf[2] - mb[2], xf[3] - mb[3]);
        u32 u01 = pk_bf(bf_lo(d01) * rs[0], bf_hi(d01) * rs[1]), u23 = pk_bf(bf_lo(d23) * rs[2], bf_hi(d23) * rs[3]);
        float uu[4] = {bf_lo(u01), bf_hi(u01), bf_lo(u23), bf_hi(u23)};
        u32 s01 = pk_bf(uu[0] * ds[ch], uu[1] * ds[ch]), s23 = pk_bf(uu[2] * ds[ch], uu[3] * ds[ch]);
        *(u32x2*)(U + ch * UP + 512 + t * 8) = u32x2{pk_h(uu[0], uu[1]), pk_h(uu[2], uu[3])};
        *(u32x4*)(XU + ch * 8192 + t * 16) = u32x4{xv[ch][0], s01, xv[ch][1], s23};
    }
    STAMP(2);
    lds_barrier();
    STAMP(3);
    if (w < 4) {
        const int ch = w >> 1, mh = w & 1;
        f32x4 acc[8];
#pragma unroll
        for (int i = 0; i < 8; ++i) acc[i] = f32x4{0.f, 0.f, 0.f, 0.f};
        const u32 lb = (u32)(uintptr_t)(__attribute__((address_space(3))) char*)sm;
        const u32 fb = lb + (ch * 4 + 3 - (r & 3)) * RCP + 2 * (2028 - 32 * (4 * mh) - 256 * 7 - (r & ~3) + 8 * g) - 192;
        const u32 ub = lb + 8 * RCP + ch * UP + 2 * (256 - 32 * (4 * mh) - 16 + 16 * r + 8 * g) - 192;
        struct CF { u32x2 lo[8], hi[8]; u32x4 b[8]; };
        CF c0f, c1f;
#define C_RDF(F, MU, I) do { \
        asm volatile("ds_read_b64 %0, %1 offset:%2" : "=v"(F.lo[I]) : "v"(fb), "n"(192 - 64 * (MU) + 512 * (7 - (I)))); \
        asm volatile("ds_read_b64 %0, %1 offset:%2" : "=v"(F.hi[I]) : "v"(fb), "n"(200 - 64 * (MU) + 512 * (7 - (I)))); } while (0)
#define C_RDB(F, MU, I) asm volatile("ds_read_b128 %0, %1 offset:%2" : "=v"(F.b[I]) : "v"(ub), "n"(192 - 64 * (MU) + 512 * (I)))
#define C_WAIT(F) do { asm volatile("s_waitcnt lgkmcnt(0)" : "+v"(F.lo[0]), "+v"(F.lo[1]), "+v"(F.lo[2]), "+v"(F.lo[3]), "+v"(F.lo[4]), "+v"(F.lo[5]), \
        "+v"(F.lo[6]), "+v"(F.lo[7]), "+v"(F.hi[0]), "+v"(F.hi[1]), "+v"(F.hi[2]), "+v"(F.hi[3]), "+v"(F.hi[4]), "+v"(F.hi[5]), "+v"(F.hi[6]), "+v"(F.hi[7])); \
        asm volatile("" : "+v"(F.b[0]), "+v"(F.b[1]), "+v"(F.b[2]), "+v"(F.b[3]), "+v"(F.b[4]), "+v"(F.b[5]), "+v"(F.b[6]), "+v"(F.b[7])); } while (0)
#define C_FR(F, I) __builtin_bit_cast(f16x8, u32x4{F.lo[I][0], F.lo[I][1], F.hi[I][0], F.hi[I][1]})
#define C_STEP(CUR, NXT, MUN, PF) do { \
        _Pragma("unroll") for (int a = 0; a < 8; ++a) { \
            _Pragma("unroll") for (int q = 7; q >= 0; --q) { \
                if (q < 8 - a) { \
                    const int slot = a * 8 - a * (a - 1) / 2 + (7 - a - q); \
                    if (slot % 5 == 0) { \
                        __builtin_amdgcn_sched_barrier(0); \
                        if (PF) { C_RDF(NXT, MUN, slot / 5); C_RDB(NXT, MUN, slot / 5); } \
                    } \
                    acc[a + q] = mfma_h(C_FR(CUR, a), __builtin_bit_cast(f16x8, CUR.b[q]), acc[a + q]); \
                } } } \
        __builtin_amdgcn_sched_barrier(0); } while (0)
#pragma unroll
        for (int i = 0; i < 8; ++i) { C_RDF(c0f, 0, i); C_RDB(c0f, 0, i); }
        C_WAIT(c0f);
        __builtin_amdgcn_sched_barrier(0);
        C_STEP(c0f, c1f, 1, true);  C_WAIT(c1f);
        C_STEP(c1f, c0f, 2, true);  C_WAIT(c0f);
        C_STEP(c0f, c1f, 3, true);  C_WAIT(c1f);
        C_STEP(c1f, c0f, 0, false);
#undef C_RDF
#undef C_RDB
#undef C_WAIT
#undef C_FR
#undef C_STEP
        STAMP(4);
#pragma unroll
        for (int i = 0; i < 8; ++i) *(f32x4*)(RED + ((ch * 2 + mh) * 8 + i) * 1024 + lane * 16) = acc[i];
    }
    lds_barrier();
    STAMP(5);
    const int ch = w >> 2;
    u16* dst = xo + (size_t)(c0 + ch) * XP;
#pragma unroll
    for (int i = 0; i < 2; ++i) {
        const int tt = 2 * (w & 3) + i;
        f32x4 y = (*(const f32x4*)(RED + (ch * 16 + tt) * 1024 + lane * 16) + *(const f32x4*)(RED + (ch * 16 + 8 + tt) * 1024 + lane * 16)) * ksc;
        int l = 256 * tt + 16 * r + 4 * g;
        u32x4 xu = *(const u32x4*)(XU + ch * 8192 + l * 4);
        u32 y01 = pk_bf(y[0], y[1]), y23 = pk_bf(y[2], y[3]);
        u32 z01 = pk_bf(bf_lo(y01) + bf_lo(xu[1]), bf_hi(y01) + bf_hi(xu[1])), z23 = pk_bf(bf_lo(y23) + bf_lo(xu[3]), bf_hi(y23) + bf_hi(xu[3]));
        st8(dst + l, u32x2{pk_bf(bf_lo(xu[0]) + bf_lo(z01), bf_hi(xu[0]) + bf_hi(z01)),
                           pk_bf(bf_lo(xu[2]) + bf_lo(z23), bf_hi(xu[2]) + bf_hi(z23))});
    }
    STAMP_END();
}

constexpr int QSLOT = 28672, QNS = 5;
DEVI u32 phi2(f32x2 a) {
    const u32 tb = pk_bf(a[0], a[1]);
    const f32x2 tq = {bf_lo(tb), bf_hi(tb)};
    const f32x2 mn = {__builtin_amdgcn_fmed3f(tq[0], -__builtin_inff(), 0.f), __builtin_amdgcn_fmed3f(tq[1], -__builtin_inff(), 0.f)};
    const f32x2 me = mn * 1.4426950408889634f;
    const f32x2 p = f32x2{__builtin_amdgcn_exp2f(me[0]), __builtin_amdgcn_exp2f(me[1])} + (tq - mn);
    const u32 pb = pk_bf(p[0], p[1]);
    const f32x2 p2 = f32x2{bf_lo(pb), bf_hi(pb)} + 1e-6f;
    return pk_bf(p2[0], p2[1]);
}
template <int N> DEVI void wait_vm() { asm volatile("s_waitcnt vmcnt(%0)" ::"n"(N) : "memory"); }

__global__ __launch_bounds__(512, 1) void qkv_kernel(
    const u16* __restrict__ xT, const u16* __restrict__ wqF, const float* __restrict__ Wsum,
    u16* __restrict__ pqF, float* __restrict__ kvacc DBG_ARG) {
    STAMP(0);
    __shared__ __attribute__((aligned(16))) char ring[QNS * QSLOT + 4096];
    float* lnm = (float*)(ring + QNS * QSLOT);
    float* lnr = lnm + 128;
    float* wst = lnr + 128;
    const int t = threadIdx.x, lane = t & 63, g = lane >> 4, j = lane & 15;
    const int w = __builtin_amdgcn_readfirstlane(t >> 6), rg = (w >> 1) & 1, hh = w & 1;
    const int xcd = blockIdx.x & 7, jb = blockIdx.x >> 3;
    const int rt = 4 * (xcd >> 1) + (jb & 3), hp = 8 * (xcd & 1) + (jb >> 2);
    f32x4 aq[4], ak[4], av[4];
    char* hand = ring;
    if (w >= 4) {
        const int lw = w - 4;
        const u16* xs[4];
#pragma unroll
        for (int i = 0; i < 4; ++i) {
            int row = 4 * (4 * lw + i) + g, c16 = j;
            int f = (row & 3) | (((row >> 3) & 1) << 2);
            xs[i] = xT + (size_t)row * XP + rt * 128 + ((c16 >> 1) ^ f) * 16 + (c16 & 1) * 8;
        }
        const u16* wsrc = wqF + (size_t)hp * (16 * 6 * 512) + (3 * lw) * 512 + lane * 8;
        auto issue = [&](int kc) {
            char* slot = ring + (kc % QNS) * QSLOT;
#pragma unroll
            for (int i = 0; i < 4; ++i)
                __builtin_amdgcn_global_load_lds((const u32*)(xs[i] + (size_t)kc * 64 * XP), (u32*)(slot + (4 * lw + i) * 1024), 16, 0, 0);
#pragma unroll
            for (int i = 0; i < 3; ++i)
                __builtin_amdgcn_global_load_lds((const u32*)(wsrc + kc * (12 * 512) + i * 512), (u32*)(slot + 16384 + (3 * lw + i) * 1024), 16, 0, 0);
        };
        issue(0); issue(1);
        wait_vm<7>();
        __builtin_amdgcn_s_barrier();
        issue(2);
#pragma unroll
        for (int kc = 1; kc < 8; ++kc) {
            if (kc + 2 < 8) issue(kc + 2);
            if (kc <= 5) wait_vm<14>(); else if (kc == 6) wait_vm<7>(); else wait_vm<0>();
            __builtin_amdgcn_s_barrier();
        }
        STAMP(2);
    } else {
        if (w == 0) {
            const int h2 = lane >= 48 ? 0 : lane;
            if (lane < 48) {
                const float* src = Wsum + (h2 / 16) * DM + 2 * hp * 16 + (h2 & 15);
                constexpr int P = 3 * DM;
                wst[h2] = (src[0] + src[P]) + (src[2 * P] + src[3 * P]);
                wst[48 + h2] = (src[16] + src[P + 16]) + (src[2 * P + 16] + src[3 * P + 16]);
            }
        }
        f32x4 as[4];
#pragma unroll
        for (int i = 0; i < 4; ++i) { aq[i] = ak[i] = av[i] = as[i] = f32x4{0.f, 0.f, 0.f, 0.f}; }
        const u32 ones = 0x3f803f80u;
        const bf16x8 onesv = __builtin_bit_cast(bf16x8, u32x4{ones, ones, ones, ones});
        const int q4 = (lane >> 2) & 3, fl = q4 | ((g & 1) << 2);
        const u32 lbase = (u32)(uintptr_t)(__attribute__((address_space(3))) char*)ring;
        u32 xoff[4];
#pragma unroll
        for (int mt = 0; mt < 4; ++mt) xoff[mt] = lbase + (8 * g + q4) * 256 + (((4 * rg + mt) ^ fl) * 32) + (lane & 3) * 8;
        const u32 woff = lbase + 16384 + hh * 3 * 1024 + lane * 16;
        struct Frag { u32x2 lo[4], hi[4]; u32x4 wq, wk, wv; };
        auto rd = [&](Frag& f, int kc, int ks) {
            const u32 so = (kc % QNS) * QSLOT;
#pragma unroll
            for (int mt = 0; mt < 4; ++mt) {
                u32 a = xoff[mt] + so;
                if (ks == 0) {
                    asm volatile("ds_read_b64_tr_b16 %0, %1" : "=v"(f.lo[mt]) : "v"(a));
                    asm volatile("ds_read_b64_tr_b16 %0, %1 offset:1024" : "=v"(f.hi[mt]) : "v"(a));
                } else {
                    asm volatile("ds_read_b64_tr_b16 %0, %1 offset:8192" : "=v"(f.lo[mt]) : "v"(a));
                    asm volatile("ds_read_b64_tr_b16 %0, %1 offset:9216" : "=v"(f.hi[mt]) : "v"(a));
                }
            }
            u32 a = woff + so;
            if (ks == 0) {
                asm volatile("ds_read_b128 %0, %1" : "=v"(f.wq) : "v"(a));
                asm volatile("ds_read_b128 %0, %1 offset:1024" : "=v"(f.wk) : "v"(a));
                asm volatile("ds_read_b128 %0, %1 offset:2048" : "=v"(f.wv) : "v"(a));
            } else {
                asm volatile("ds_read_b128 %0, %1 offset:6144" : "=v"(f.wq) : "v"(a));
                asm volatile("ds_read_b128 %0, %1 offset:7168" : "=v"(f.wk) : "v"(a));
                asm volatile("ds_read_b128 %0, %1 offset:8192" : "=v"(f.wv) : "v"(a));
            }
        };
#define Q_WAIT(F, N) asm volatile("s_waitcnt lgkmcnt(" #N ")" : "+v"(F.lo[0]), "+v"(F.hi[0]), "+v"(F.lo[1]), "+v"(F.hi[1]), \
        "+v"(F.lo[2]), "+v"(F.hi[2]), "+v"(F.lo[3]), "+v"(F.hi[3]), "+v"(F.wq), "+v"(F.wk), "+v"(F.wv) :: "memory")
        auto mm = [&](const Frag& f) {
            bf16x8 wq = __builtin_bit_cast(bf16x8, f.wq), wk = __builtin_bit_cast(bf16x8, f.wk), wv = __builtin_bit_cast(bf16x8, f.wv);
            bf16x8 xa[4];
#pragma unroll
            for (int mt = 0; mt < 4; ++mt) xa[mt] = __builtin_bit_cast(bf16x8, u32x4{f.lo[mt][0], f.lo[mt][1], f.hi[mt][0], f.hi[mt][1]});
#pragma unroll
            for (int mt = 0; mt < 4; ++mt) {
                aq[mt] = mfma_bf(wq, xa[mt], aq[mt]);
                ak[mt] = mfma_bf(xa[mt], wk, ak[mt]);
                av[mt] = mfma_bf(xa[mt], wv, av[mt]);
            }
            if (hh) {
#pragma unroll
                for (int i = 0; i < 2; ++i) { as[i] = mfma_bf(xa[2 + i], xa[2 + i], as[i]); as[2 + i] = mfma_bf(xa[2 + i], onesv, as[2 + i]); }
            } else {
#pragma unroll
                for (int i = 0; i < 2; ++i) { as[i] = mfma_bf(xa[i], xa[i], as[i]); as[2 + i] = mfma_bf(xa[i], onesv, as[2 + i]); }
            }
        };
        Frag fa, fb;
        asm volatile("s_waitcnt vmcnt(0) lgkmcnt(0)" ::: "memory");
        __builtin_amdgcn_s_barrier();
        asm volatile("" ::: "memory");
        STAMP(1);
        rd(fa, 0, 0);
#pragma unroll
        for (int kc = 0; kc < 8; ++kc) {
            rd(fb, kc, 1);
            Q_WAIT(fa, 11);
            mm(fa);
            Q_WAIT(fb, 0);
            if (kc < 7) {
                __builtin_amdgcn_s_barrier();
                asm volatile("" ::: "memory");
                STAMP(3 + kc);
                rd(fa, kc + 1, 0);
            }
            mm(fb);
        }
#undef Q_WAIT
        STAMP(10);
#pragma unroll
        for (int i = 0; i < 2; ++i) {
            *(f32x4*)(hand + ((w * 6 + 3 * i) * 64 + lane) * 16) = aq[2 + i];
            *(f32x4*)(hand + ((w * 6 + 3 * i + 1) * 64 + lane) * 16) = ak[2 + i];
            *(f32x4*)(hand + ((w * 6 + 3 * i + 2) * 64 + lane) * 16) = av[2 + i];
        }
#pragma unroll
        for (int i = 0; i < 2; ++i) {
            const int rgi = j & 3;
            const float sq = rgi == 0 ? as[i][0] : (rgi == 1 ? as[i][1] : (rgi == 2 ? as[i][2] : as[i][3]));
            const float meanj = (rgi == 0 ? as[2 + i][0] : (rgi == 1 ? as[2 + i][1] : (rgi == 2 ? as[2 + i][2] : as[2 + i][3]))) * (1.0f / DM);
            float mb, rs;
            ln_scalars(meanj, fmaxf(fmaf(-meanj, meanj, sq * (1.0f / DM)), 0.f), mb, rs);
            if (g == (j >> 2)) { lnm[rg * 64 + (2 * hh + i) * 16 + j] = mb; lnr[rg * 64 + (2 * hh + i) * 16 + j] = rs; }
        }
    }
    lds_barrier();
    if (w >= 4) {
#pragma unroll
        for (int i = 0; i < 2; ++i) {
            aq[i] = *(const f32x4*)(hand + (((w - 4) * 6 + 3 * i) * 64 + lane) * 16);
            ak[i] = *(const f32x4*)(hand + (((w - 4) * 6 + 3 * i + 1) * 64 + lane) * 16);
            av[i] = *(const f32x4*)(hand + (((w - 4) * 6 + 3 * i + 2) * 64 + lane) * 16);
        }
    }
    STAMP(11);
    const int mt0 = (w >> 2) * 2;
    const f32x4 wsq = *(const f32x4*)(wst + hh * 48 + 4 * g);
    const float wsk = wst[hh * 48 + 16 + j], wsv = wst[hh * 48 + 32 + j];
    u32 pk8[4], vv8[4];
#pragma unroll
    for (int i = 0; i < 2; ++i) {
        const int r0 = rg * 64 + (mt0 + i) * 16;
        const float m1 = lnm[r0 + j], r1 = lnr[r0 + j];
        const u32 q01 = phi2((f32x2{aq[i][0], aq[i][1]} - f32x2{wsq[0], wsq[1]} * m1) * r1);
        const u32 q23 = phi2((f32x2{aq[i][2], aq[i][3]} - f32x2{wsq[2], wsq[3]} * m1) * r1);
        const int lt = rt * 8 + rg * 4 + mt0 + i;
        st8(pqF + ((size_t)(lt * 16 + hp) * 64 + lane) * 8 + hh * 4, u32x2{q01, q23});
        const f32x4 mr = *(const f32x4*)(lnm + r0 + 4 * g), rr = *(const f32x4*)(lnr + r0 + 4 * g);
        const f32x2 m01 = {mr[0], mr[1]}, m23 = {mr[2], mr[3]}, r01 = {rr[0], rr[1]}, r23 = {rr[2], rr[3]};
        pk8[2 * i] = phi2((f32x2{ak[i][0], ak[i][1]} - m01 * wsk) * r01);
        pk8[2 * i + 1] = phi2((f32x2{ak[i][2], ak[i][3]} - m23 * wsk) * r23);
        const f32x2 v01 = (f32x2{av[i][0], av[i][1]} - m01 * wsv) * r01, v23 = (f32x2{av[i][2], av[i][3]} - m23 * wsv) * r23;
        vv8[2 * i] = pk_bf(v01[0], v01[1]); vv8[2 * i + 1] = pk_bf(v23[0], v23[1]);
    }
    const u32 ones = 0x3f803f80u;
    const bf16x8 pk = __builtin_bit_cast(bf16x8, u32x4{pk8[0], pk8[1], pk8[2], pk8[3]});
    const bf16x8 vv = __builtin_bit_cast(bf16x8, u32x4{vv8[0], vv8[1], vv8[2], vv8[3]});
    const f32x4 z4 = {0.f, 0.f, 0.f, 0.f};
    const f32x4 kvT = mfma_bf(vv, pk, z4);
    const f32x4 zz = mfma_bf(__builtin_bit_cast(bf16x8, u32x4{ones, ones, ones, ones}), pk, z4);
    STAMP(12);
    float* red = (float*)(ring + QSLOT);
    float* rp = red + (hh * 4 + (w >> 1)) * KVH;
#pragma unroll
    for (int e = 0; e < 4; ++e) rp[(4 * g + e) * 16 + j] = kvT[e];
    if (g == 0) rp[256 + j] = zz[0];
    lds_barrier();
    for (int i = t; i < 2 * KVH; i += 512) {
        int h2 = i >= KVH, ii = i - h2 * KVH;
        const float* rq = red + h2 * 4 * KVH + ii;
        unsafeAtomicAdd(kvacc + (2 * hp + h2) * KVH + ii, (rq[0] + rq[KVH]) + (rq[2 * KVH] + rq[3 * KVH]));
    }
    STAMP_END();
}

template <int FINAL>
__global__ __launch_bounds__(512, 1) void oproj_kernel(
    const u16* __restrict__ xT, const u16* __restrict__ pqF, const float* __restrict__ kvacc,
    const u16* __restrict__ woF, u16* __restrict__ xo, f32x2* __restrict__ stats DBG_ARG) {
    STAMP(0);
    __shared__ __attribute__((aligned(16))) char sm[32768 + 128 * 72 + 32 * 272 + 2048];
    char* OF = sm; char* XT = sm + 32768; char* OT = XT + 128 * 72; f32x2* ST = (f32x2*)(OT + 32 * 272);
    const int t = threadIdx.x, lane = t & 63, w = t >> 6, g = lane >> 4, j = lane & 15;
    const int xcd = blockIdx.x & 7, jb = blockIdx.x >> 3;
    const int rt = xcd * 8 + (jb >> 2), cq = jb & 3, l0 = rt * 32;
    u32x4 pq[2][2];
#pragma unroll
    for (int lt = 0; lt < 2; ++lt)
#pragma unroll
        for (int i = 0; i < 2; ++i) pq[lt][i] = *(const u32x4*)(pqF + ((size_t)((2 * rt + lt) * 16 + 2 * w + i) * 64 + lane) * 8);
    f32x4 kf[4], zf[4];
#pragma unroll
    for (int i = 0; i < 4; ++i) {
        kf[i] = *(const f32x4*)(kvacc + (4 * w + i) * KVH + j * 16 + 4 * g);
        zf[i] = *(const f32x4*)(kvacc + (4 * w + i) * KVH + 256 + 4 * g);
    }
    u32x4 xin = *(const u32x4*)(xT + (size_t)(cq * 128 + (t >> 2)) * XP + l0 + (t & 3) * 8);
    bf16x8 wf[16];
#pragma unroll
    for (int ks = 0; ks < 16; ++ks) wf[ks] = *(const bf16x8*)(woF + ((size_t)((cq * 8 + w) * 16 + ks) * 64 + lane) * 8);
    {
        char* p = XT + (t >> 2) * 72 + (t & 3) * 16;
        *(u32x2*)p = u32x2{xin[0], xin[1]}; *(u32x2*)(p + 8) = u32x2{xin[2], xin[3]};
    }
#pragma unroll
    for (int i = 0; i < 2; ++i) {
        u32x4 of[2];
#pragma unroll
        for (int h2 = 0; h2 < 2; ++h2) {
            f32x4 k4 = kf[2 * i + h2], z4 = zf[2 * i + h2];
            s16x4 ka = __builtin_bit_cast(s16x4, u32x2{pk_bf(k4[0], k4[1]), pk_bf(k4[2], k4[3])});
            s16x4 za = __builtin_bit_cast(s16x4, u32x2{pk_bf(z4[0], z4[1]), pk_bf(z4[2], z4[3])});
#pragma unroll
            for (int lt = 0; lt < 2; ++lt) {
                s16x4 b = __builtin_bit_cast(s16x4, u32x2{pq[lt][i][2 * h2], pq[lt][i][2 * h2 + 1]});
                f32x4 zero = {0.f, 0.f, 0.f, 0.f};
                f32x4 num = mfma_bf16k(ka, b, zero), den = mfma_bf16k(za, b, zero);
                float rd = __builtin_amdgcn_rcpf(rbf(rbf(den[0]) + 1e-6f));
                const u32 n01 = pk_bf(num[0], num[1]), n23 = pk_bf(num[2], num[3]);
                const f32x2 o01 = f32x2{bf_lo(n01), bf_hi(n01)} * rd, o23 = f32x2{bf_lo(n23), bf_hi(n23)} * rd;
                of[lt][2 * h2] = pk_bf(o01[0], o01[1]);
                of[lt][2 * h2 + 1] = pk_bf(o23[0], o23[1]);
            }
        }
#pragma unroll
        for (int lt = 0; lt < 2; ++lt) *(u32x4*)(OF + (lt * 16 + 2 * w + i) * 1024 + lane * 16) = of[lt];
    }
    STAMP(1);
    lds_barrier();
    STAMP(2);
    f32x4 acc[2] = {{0.f, 0.f, 0.f, 0.f}, {0.f, 0.f, 0.f, 0.f}};
#pragma unroll
    for (int ks = 0; ks < 16; ++ks)
#pragma unroll
        for (int lt = 0; lt < 2; ++lt)
            acc[lt] = mfma_bf(wf[ks], *(const bf16x8*)(OF + (lt * 16 + ks) * 1024 + lane * 16), acc[lt]);
    STAMP(3);
#pragma unroll
    for (int lt = 0; lt < 2; ++lt) {
        float r[4];
#pragma unroll
        for (int e = 0; e < 4; ++e) {
            u16* px = (u16*)(XT + (16 * w + 4 * g + e) * 72) + 16 * lt + j;
            r[e] = rbf(bf2f(*px) + rbf(acc[lt][e]));
            if (!FINAL) *px = (u16)(__float_as_uint(r[e]) >> 16);
        }
        if (FINAL) {
            *(u32x2*)(OT + (16 * lt + j) * 272 + (16 * w + 4 * g) * 2) = u32x2{pk_bf(r[0], r[1]), pk_bf(r[2], r[3])};
        } else {
            float s = (r[0] + r[1]) + (r[2] + r[3]);
            s = quad_rows_sum(s);
            float mean = s * (1.0f / 16.0f), q = 0.f;
#pragma unroll
            for (int e = 0; e < 4; ++e) { float d = r[e] - mean; q = fmaf(d, d, q); }
            q = quad_rows_sum(q);
            if (g == 0) ST[w * 32 + 16 * lt + j] = f32x2{mean, q};
        }
    }
    lds_barrier();
    if (FINAL) {
        u32x4 o = *(const u32x4*)(OT + (t >> 4) * 272 + (t & 15) * 16);
        st16(xo + (size_t)(l0 + (t >> 4)) * DM + cq * 128 + (t & 15) * 8, o);
    } else {
        const char* p = XT + (t >> 2) * 72 + (t & 3) * 16;
        u32x2 a = *(const u32x2*)p, c = *(const u32x2*)(p + 8);
        st16(xo + (size_t)(cq * 128 + (t >> 2)) * XP + l0 + (t & 3) * 8, u32x4{a[0], a[1], c[0], c[1]});
        if (t < 32) {
            float mean = 0.f, m2 = 0.f;
#pragma unroll
            for (int i = 0; i < 8; ++i) { f32x2 s = ST[i * 32 + t]; mean += s[0]; m2 += s[1]; }
            mean *= 0.125f;
#pragma unroll
            for (int i = 0; i < 8; ++i) { float d = ST[i * 32 + t][0] - mean; m2 = fmaf(16.0f * d, d, m2); }
            stats[cq * SL + l0 + t] = f32x2{mean, m2};
        }
    }
    STAMP_END();
}

extern "C" void kernel_launch(void* const* d_in, const int* in_sizes, int n_in,
                              void* d_out, int out_size, void* d_ws, size_t ws_size,
                              hipStream_t stream) {
    const u16* u = (const u16*)d_in[0];
    const u16* lar = (const u16*)d_in[1];
    const u16* aim = (const u16*)d_in[2];
    const u16* cre = (const u16*)d_in[3];
    const u16* cim = (const u16*)d_in[4];
    const u16* ldt = (const u16*)d_in[5];
    const u16* dsk = (const u16*)d_in[6];
    const u16* wqkv = (const u16*)d_in[8];
    const u16* wo = (const u16*)d_in[9];
    char* ws = (char*)d_ws;
    const size_t MB = 1 << 20;
    u16* xT0 = (u16*)(ws + 18 * MB);
    u16* xT1 = (u16*)(ws + 21 * MB);
    u16* xT2 = (u16*)(ws + 24 * MB);
    u16* xT3 = (u16*)(ws + 27 * MB);
    u16* pqF = (u16*)(ws + 8 * MB);
    u16* Kr = (u16*)(ws + 10 * MB);
    u16* wqF = (u16*)(ws + 13 * MB);
    u16* woF = (u16*)(ws + 15 * MB);
    f32x2* st0 = (f32x2*)(ws + 16 * MB);
    f32x2* st2 = (f32x2*)(ws + 16 * MB + 65536);
    float* Kscale = (float*)(ws + 16 * MB + 196608);
    float* Wsum = (float*)(ws + 16 * MB + 200704);
    float* kvacc = (float*)(ws + 16 * MB + 262144);
    prep_kernel<<<P_TR + P_KG, 256, 0, stream>>>(u, lar, aim, cre, cim, ldt, wqkv, wo, xT0, st0, Kr, Kscale,
                                                               wqF, Wsum, woF, kvacc DBG_PASS(0));
    conv_kernel<1><<<256, 512, 0, stream>>>(xT0, st0, Kr, Kscale, dsk, xT1 DBG_PASS(1));
    qkv_kernel<<<256, 512, 0, stream>>>(xT1, wqF, Wsum, pqF, kvacc DBG_PASS(2));
    oproj_kernel<0><<<256, 512, 0, stream>>>(xT1, pqF, kvacc, woF, xT2, st2 DBG_PASS(3));
    conv_kernel<4><<<256, 512, 0, stream>>>(xT2, st2, Kr, Kscale, dsk, xT3 DBG_PASS(4));
    qkv_kernel<<<256, 512, 0, stream>>>(xT3, wqF, Wsum, pqF, kvacc + 32 * KVH DBG_PASS(5));
    oproj_kernel<1><<<256, 512, 0, stream>>>(xT3, pqF, kvacc + 32 * KVH, woF, (u16*)d_out, st2 DBG_PASS(6));
}
```

```cpp
#include <hip/hip_runtime.h>
#include <hip/hip_bf16.h>
#include <stdint.h>

typedef unsigned short u16;
typedef unsigned int u32;
typedef __attribute__((ext_vector_type(4))) float f32x4;
typedef __attribute__((ext_vector_type(2))) float f32x2;
typedef __attribute__((ext_vector_type(8))) __bf16 bf16x8;
typedef __attribute__((ext_vector_type(2))) __bf16 bf16x2;
typedef __attribute__((ext_vector_type(8))) _Float16 f16x8;
typedef __attribute__((ext_vector_type(2))) _Float16 f16x2;
typedef __attribute__((ext_vector_type(4))) short s16x4;
typedef __attribute__((ext_vector_type(8))) short s16x8;
typedef __attribute__((ext_vector_type(4))) u32 u32x4;
typedef __attribute__((ext_vector_type(2))) u32 u32x2;

#define DEVI __device__ __forceinline__

constexpr int SL = 2048;
constexpr int DM = 512;
constexpr int NST = 128;
constexpr int XP = 2176;
constexpr int KRL = 2056;
constexpr int KVH = 272;

DEVI float bf_lo(u32 v) { return __uint_as_float(v << 16); }
DEVI float bf_hi(u32 v) { return __uint_as_float(v & 0xffff0000u); }
DEVI float bf2f(u16 v) { return __uint_as_float(((u32)v) << 16); }
DEVI u32 pk_bf(float a, float b) {
    f32x2 x = {a, b};
    return __builtin_bit_cast(u32, __builtin_convertvector(x, bf16x2));
}
DEVI u32 pk_h(float a, float b) {
    f32x2 x = {a, b};
    return __builtin_bit_cast(u32, __builtin_convertvector(x, f16x2));
}
DEVI float rbf(float a) { return bf_lo(pk_bf(a, 0.f)); }
template <int C> DEVI float dpp_f(float x) {
    return __int_as_float(__builtin_amdgcn_update_dpp(0, __float_as_int(x), C, 0xf, 0xf, true));
}
DEVI float row16_sum(float x) {
    x += dpp_f<0xB1>(x); x += dpp_f<0x4E>(x); x += dpp_f<0x141>(x); x += dpp_f<0x140>(x);
    return x;
}
DEVI float quad_rows_sum(float x) {
    u32x2 a = __builtin_amdgcn_permlane16_swap(__float_as_uint(x), __float_as_uint(x), false, false);
    x = __uint_as_float(a[0]) + __uint_as_float(a[1]);
    u32x2 b = __builtin_amdgcn_permlane32_swap(__float_as_uint(x), __float_as_uint(x), false, false);
    return __uint_as_float(b[0]) + __uint_as_float(b[1]);
}
DEVI float rdl(float x, int l) { return __int_as_float(__builtin_amdgcn_readlane(__float_as_int(x), l)); }
DEVI float wave_sum(float x) {
    x = row16_sum(x);
    return (rdl(x, 0) + rdl(x, 16)) + (rdl(x, 32) + rdl(x, 48));
}
DEVI float wave_max(float x) {
    x = fmaxf(x, dpp_f<0xB1>(x)); x = fmaxf(x, dpp_f<0x4E>(x));
    x = fmaxf(x, dpp_f<0x141>(x)); x = fmaxf(x, dpp_f<0x140>(x));
    return fmaxf(fmaxf(rdl(x, 0), rdl(x, 16)), fmaxf(rdl(x, 32), rdl(x, 48)));
}
DEVI f32x4 mfma_bf(bf16x8 a, bf16x8 b, f32x4 c) { return __builtin_amdgcn_mfma_f32_16x16x32_bf16(a, b, c, 0, 0, 0); }
DEVI f32x4 mfma_h(f16x8 a, f16x8 b, f32x4 c) { return __builtin_amdgcn_mfma_f32_16x16x32_f16(a, b, c, 0, 0, 0); }
DEVI f32x4 mfma_bf16k(s16x4 a, s16x4 b, f32x4 c) { return __builtin_amdgcn_mfma_f32_16x16x16bf16_1k(a, b, c, 0, 0, 0); }
DEVI void st16(void* p, u32x4 v) {
    asm volatile("global_store_dwordx4 %0, %1, off sc1\n\ts_nop 1" ::"v"(p), "v"(v) : "memory");
}
DEVI void st8(void* p, u32x2 v) {
    __hip_atomic_store((unsigned long long*)p, __builtin_bit_cast(unsigned long long, v), __ATOMIC_RELAXED, __HIP_MEMORY_SCOPE_AGENT);
}
DEVI void lds_barrier() {
    asm volatile("s_waitcnt lgkmcnt(0)" ::: "memory");
    __builtin_amdgcn_s_barrier();
    asm volatile("" ::: "memory");
}
DEVI void ln_scalars(float mean, float var, float& mb, float& rs) {
    mb = rbf(mean);
    float sb = rbf(__builtin_amdgcn_sqrtf(rbf(rbf(var) + 1e-5f)));
    rs = __builtin_amdgcn_rcpf(sb);
}

constexpr int P_TR = 256, P_KG = 512;

__global__ __launch_bounds__(256, 2) void prep_kernel(
    const u16* __restrict__ x, const u16* __restrict__ lar, const u16* __restrict__ aim,
    const u16* __restrict__ cre, const u16* __restrict__ cim, const u16* __restrict__ ldt,
    const u16* __restrict__ wqkv, const u16* __restrict__ wo,
    u16* __restrict__ xT, f32x2* __restrict__ stats, u16* __restrict__ Kr, float* __restrict__ Kscale,
    u16* __restrict__ wqF, float* __restrict__ Wsum, u16* __restrict__ woF, float* __restrict__ kvacc) {
    __shared__ __attribute__((aligned(16))) char sm[24576];
    const int t = threadIdx.x, lane = t & 63, w = __builtin_amdgcn_readfirstlane(t >> 6), g = lane >> 4, j = lane & 15;
    int b = blockIdx.x < P_KG ? blockIdx.x + P_TR : blockIdx.x - P_KG;
    if (b < P_TR) {
        const int r0 = ((b & 7) * 32 + (b >> 3)) * 8;
        u32 v[8];
        float pv[8];
#pragma unroll
        for (int i = 0; i < 8; ++i) {
            v[i] = *(const u32*)(x + (size_t)(r0 + i) * DM + 2 * t);
            pv[i] = bf2f(x[(size_t)(r0 + i) * DM]);
        }
        u16* dst = xT + (size_t)(2 * t) * XP + r0;
        *(u32x4*)dst = u32x4{__builtin_amdgcn_perm(v[1], v[0], 0x05040100), __builtin_amdgcn_perm(v[3], v[2], 0x05040100),
                             __builtin_amdgcn_perm(v[5], v[4], 0x05040100), __builtin_amdgcn_perm(v[7], v[6], 0x05040100)};
        *(u32x4*)(dst + XP) = u32x4{__builtin_amdgcn_perm(v[1], v[0], 0x07060302), __builtin_amdgcn_perm(v[3], v[2], 0x07060302),
                                    __builtin_amdgcn_perm(v[5], v[4], 0x07060302), __builtin_amdgcn_perm(v[7], v[6], 0x07060302)};
        float u8[8];
#pragma unroll
        for (int i = 0; i < 8; ++i) {
            const float d0 = bf_lo(v[i]) - pv[i], d1 = bf_hi(v[i]) - pv[i];
            u32x2 sw = __builtin_amdgcn_permlane32_swap(__float_as_uint(d0 + d1), __float_as_uint(fmaf(d0, d0, d1 * d1)), false, false);
            u8[i] = __uint_as_float(sw[0]) + __uint_as_float(sw[1]);
        }
        float* red = (float*)sm;
#pragma unroll
        for (int i = 0; i < 4; ++i) {
            u32x2 sw = __builtin_amdgcn_permlane16_swap(__float_as_uint(u8[i]), __float_as_uint(u8[i + 4]), false, false);
            const float tot = row16_sum(__uint_as_float(sw[0]) + __uint_as_float(sw[1]));
            if (j == 0) red[w * 16 + g * 4 + i] = tot;
        }
        lds_barrier();
        if (t < 8) {
            const float S = (red[t] + red[16 + t]) + (red[32 + t] + red[48 + t]);
            const float Q = (red[8 + t] + red[24 + t]) + (red[40 + t] + red[56 + t]);
            const float p = bf2f(x[(size_t)(r0 + t) * DM]);
            float mb, rs;
            ln_scalars(fmaf(S, 1.0f / DM, p), fmaxf(fmaf(-S * (1.0f / DM), S, Q), 0.f) * (1.0f / DM), mb, rs);
            stats[r0 + t] = f32x2{mb, rs};
        }
        return;
    }
    b -= P_TR;
    {
        const int d = b;
        const u16* pc = cre + d * NST; const u16* pi = cim + d * NST;
        float cm = fmaxf(fabsf(bf2f(pc[lane])) + fabsf(bf2f(pi[lane])),
                         fabsf(bf2f(pc[lane + 64])) + fabsf(bf2f(pi[lane + 64])));
        const int ns = d * NST + 32 * w + (lane & 31);
        const u16 pl = lar[ns], pa = aim[ns], pcr = cre[ns], pci = cim[ns];
        const float dt = rbf(__expf(bf2f(ldt[d])));
        cm = wave_max(cm);
        const float cinv = cm > 0.f ? 1.0f / cm : 0.f;
        float* spar = (float*)(sm + 22528) + w * 128;
        if (lane < 32) {
            spar[lane] = -1.4426950408889634f * dt * rbf(__expf(bf2f(pl)));
            spar[32 + lane] = 0.15915494309189535f * dt * bf2f(pa);
            spar[64 + lane] = bf2f(pcr) * cinv;
            spar[96 + lane] = bf2f(pci) * cinv;
        }
        __builtin_amdgcn_sched_barrier(0);
        u16 we[8];
        const bool isq = b < 384;
        const int wnt = (b >> 2) % 6, whp = (b >> 2) / 6;
        const int wcol = (wnt % 3) * DM + (2 * whp + wnt / 3) * 16 + j;
        const int wks = 4 * (b & 3) + w, wct = (b - 384) >> 2, wh2 = 4 * w + (b & 3);
        const char* wbase = (const char*)(isq ? wqkv : wo);
        const u32 woff = 2u * (isq ? (32 * wks + 8 * g) * (3 * DM) + wcol : (32 * wh2 + 4 * g) * DM + wct * 16 + j);
        u16* wdst = (isq ? wqF : woF) + 8u * (u32)((isq ? (whp * 16 + wks) * 6 + wnt : wct * 16 + wh2) * 64 + lane);
#pragma unroll
        for (int jj = 0; jj < 8; ++jj)
            we[jj] = *(const u16*)(wbase + (woff + (u32)(isq ? jj * (6 * DM) : ((jj >> 2) * 16 + (jj & 3)) * (2 * DM))));
        if (!isq && t < 136) kvacc[(b - 384) * 136 + t] = 0.f;
        __builtin_amdgcn_sched_barrier(0);
        f32x4 acc[4];
#pragma unroll
        for (int s = 0; s < 4; ++s) acc[s] = f32x4{0.f, 0.f, 0.f, 0.f};
        const float fj = (float)j;
#pragma unroll
        for (int ts = 0; ts < 2; ++ts) {
            u32x4 fr[8];
            f32x4 pq4[4];
#pragma unroll
            for (int i = 0; i < 4; ++i) pq4[i] = *(const f32x4*)(spar + 32 * i + 16 * ts + 4 * g);
#pragma unroll
            for (int e2 = 0; e2 < 2; ++e2) {
                const f32x2 ra = {pq4[0][2 * e2], pq4[0][2 * e2 + 1]}, rb = {pq4[1][2 * e2], pq4[1][2 * e2 + 1]};
                const f32x2 fc = {pq4[2][2 * e2], pq4[2][2 * e2 + 1]}, fi = {pq4[3][2 * e2], pq4[3][2 * e2 + 1]};
                const f32x2 nfc = -fc;
                const f32x2 me = ra * fj;
                f32x2 rr = rb * fj;
                rr -= f32x2{rintf(rr[0]), rintf(rr[1])};
                const f32x2 mm = {__builtin_amdgcn_exp2f(me[0]), __builtin_amdgcn_exp2f(me[1])};
                f32x2 pr = mm * f32x2{__builtin_amdgcn_cosf(rr[0]), __builtin_amdgcn_cosf(rr[1])};
                f32x2 pi_ = mm * f32x2{__builtin_amdgcn_sinf(rr[0]), __builtin_amdgcn_sinf(rr[1])};
#pragma unroll
                for (int k = 0; k < 8; ++k) {
                    if (k > 0) {
                        f32x2 nr = pr * pr - pi_ * pi_, ni = (pr + pr) * pi_;
                        pr = nr; pi_ = ni;
                    }
                    f32x2 qr = pr, qi = pi_;
                    if (k == 7) {
                        f32x2 sr = {dpp_f<0x108>(pr[0]), dpp_f<0x108>(pr[1])}, si = {dpp_f<0x108>(pi_[0]), dpp_f<0x108>(pi_[1])};
                        f32x2 tr = sr * sr - si * si, ti = (sr + sr) * si;
                        if (j == 0) { qr = tr; qi = ti; }
                    }
                    if (k < 4) { fr[k][2 * e2] = pk_h(qr[0], qi[0]); fr[k][2 * e2 + 1] = pk_h(qr[1], qi[1]); }
                    else {
                        f32x2 br = fc * qr - fi * qi, bi = nfc * qi - fi * qr;
                        fr[k][2 * e2] = pk_h(br[0], bi[0]); fr[k][2 * e2 + 1] = pk_h(br[1], bi[1]);
                    }
                }
            }
#pragma unroll
            for (int s = 0; s < 4; ++s)
                acc[s] = mfma_h(__builtin_bit_cast(f16x8, fr[s]), __builtin_bit_cast(f16x8, fr[4 + s]), acc[s]);
        }
        __builtin_amdgcn_sched_barrier(0);
        float* part = (float*)sm;
        float* tab = part + 4096;
        float* wred = tab + 1280;
#pragma unroll
        for (int s = 0; s < 4; ++s) *(f32x4*)(part + (w * 4 + s) * 256 + j * 16 + 4 * g) = acc[s];
        {
            u32x4 o;
            float sw = 0.f;
#pragma unroll
            for (int jj = 0; jj < 4; ++jj) {
                o[jj] = (u32)we[2 * jj] | ((u32)we[2 * jj + 1] << 16);
                sw += bf2f(we[2 * jj]) + bf2f(we[2 * jj + 1]);
            }
            wred[t] = sw;
            lds_barrier();
            st16(wdst, o);
            if (isq && t < 16) {
                float a = 0.f;
#pragma unroll
                for (int i = 0; i < 16; ++i) a += wred[i * 16 + t];
                Wsum[(b & 3) * (3 * DM) + wcol] = a;
            }
        }
#pragma unroll
        for (int s = 0; s < 4; ++s) {
            const float v = (part[s * 256 + t] + part[(4 + s) * 256 + t]) + (part[(8 + s) * 256 + t] + part[(12 + s) * 256 + t]);
            tab[s * 256 + t] = v;
            if (s == 3 && t == 0) tab[1152] = v;
        }
        lds_barrier();
        float kv[8];
        {
            const int lb = 2040 - 8 * t;
            const bool big = lb >= 128;
#pragma unroll
            for (int e = 0; e < 8; e += 2) {
                const int l = lb + 7 - e;
                const u32 pb = pk_bf((float)l, (float)(l - 1));
                const u32 h0 = pb & 0xffffu, h1 = pb >> 16;
                kv[e] = tab[big ? (int)(h0 + (h0 & 0x7f80u)) - 34176 : l];
                kv[e + 1] = tab[big ? (int)(h1 + (h1 & 0x7f80u)) - 34176 : l - 1];
            }
        }
        u16* dst = Kr + (size_t)d * KRL;
        st16(dst + 8 * t, u32x4{pk_h(kv[0], kv[1]), pk_h(kv[2], kv[3]), pk_h(kv[4], kv[5]), pk_h(kv[6], kv[7])});
        if (t == 0) { *(u32x4*)(dst + 2048) = u32x4{0, 0, 0, 0}; Kscale[d] = 2.0f * dt * cm; }
    }
}

constexpr int RCP = 4160;
constexpr int UP = (256 + SL) * 2;
template <int NCT>
__global__ __launch_bounds__(512, 1) void conv_kernel(
    const u16* __restrict__ xT, const f32x2* __restrict__ stats, const u16* __restrict__ Kr,
    const float* __restrict__ Kscale, const u16* __restrict__ dskip, u16* __restrict__ xo) {
    __shared__ __attribute__((aligned(16))) char sm[8 * RCP + 2 * UP + 16384 + 32768];
    char* RC = sm; char* U = sm + 8 * RCP; char* XU = U + 2 * UP; char* RED = XU + 16384;
    const int t = threadIdx.x, lane = t & 63, g = lane >> 4, r = lane & 15;
    const int w = __builtin_amdgcn_readfirstlane(t >> 6);
    const int c0 = 2 * blockIdx.x;
    u32x2 xv[2];
#pragma unroll
    for (int ch = 0; ch < 2; ++ch) xv[ch] = *(const u32x2*)(xT + (size_t)(c0 + ch) * XP + 4 * t);
    const int kch = t >> 8, kt = t & 255;
    const u32x4 ka = *(const u32x4*)(Kr + (size_t)(c0 + kch) * KRL + 8 * kt);
    const u32x2 kb = *(const u32x2*)(Kr + (size_t)(c0 + kch) * KRL + 8 * kt + 8);
    f32x4 sv[NCT][2];
#pragma unroll
    for (int ct = 0; ct < NCT; ++ct)
#pragma unroll
        for (int i = 0; i < 2; ++i) sv[ct][i] = *(const f32x4*)(stats + ct * SL + 4 * t + 2 * i);
    const float ds[2] = {bf2f(dskip[c0]), bf2f(dskip[c0 + 1])};
    const float ksc = Kscale[c0 + (w >> 2)];
    if (t < 64) *(u32x4*)(U + (t >> 5) * UP + (t & 31) * 16) = u32x4{0, 0, 0, 0};
    else if (t < 80) *(u32x4*)(RC + ((t - 64) >> 1) * RCP + 4096 + (t & 1) * 16) = u32x4{0, 0, 0, 0};
    {
        u32 d0 = ka[0], d1 = ka[1], d2 = ka[2], d3 = ka[3], d4 = kb[0], d5 = kb[1];
        u32 a0 = __builtin_amdgcn_alignbit(d1, d0, 16), a1 = __builtin_amdgcn_alignbit(d2, d1, 16);
        u32 a2 = __builtin_amdgcn_alignbit(d3, d2, 16), a3 = __builtin_amdgcn_alignbit(d4, d3, 16);
        u32 a4 = __builtin_amdgcn_alignbit(d5, d4, 16);
        char* p = RC + kch * 4 * RCP + kt * 16;
        *(u32x4*)(p) = u32x4{d0, d1, d2, d3};
        *(u32x4*)(p + RCP) = u32x4{a0, a1, a2, a3};
        *(u32x4*)(p + 2 * RCP) = u32x4{d1, d2, d3, d4};
        *(u32x4*)(p + 3 * RCP) = u32x4{a1, a2, a3, a4};
    }
    float mb[4], rs[4];
#pragma unroll
    for (int e = 0; e < 4; ++e) {
        if (NCT == 1) { mb[e] = sv[0][e >> 1][(e & 1) * 2]; rs[e] = sv[0][e >> 1][(e & 1) * 2 + 1]; }
        else {
            float mean = 0.f, m2 = 0.f;
#pragma unroll
            for (int ct = 0; ct < NCT; ++ct) { mean += sv[ct][e >> 1][(e & 1) * 2]; m2 += sv[ct][e >> 1][(e & 1) * 2 + 1]; }
            mean *= (1.0f / NCT);
#pragma unroll
            for (int ct = 0; ct < NCT; ++ct) { float dd = sv[ct][e >> 1][(e & 1) * 2] - mean; m2 = fmaf(dd * (float)(DM / NCT), dd, m2); }
            ln_scalars(mean, m2 * (1.0f / DM), mb[e], rs[e]);
        }
    }
#pragma unroll
    for (int ch = 0; ch < 2; ++ch) {
        float xf[4] = {bf_lo(xv[ch][0]), bf_hi(xv[ch][0]), bf_lo(xv[ch][1]), bf_hi(xv[ch][1])};
        u32 d01 = pk_bf(xf[0] - mb[0], xf[1] - mb[1]), d23 = pk_bf(xf[2] - mb[2], xf[3] - mb[3]);
        u32 u01 = pk_bf(bf_lo(d01) * rs[0], bf_hi(d01) * rs[1]), u23 = pk_bf(bf_lo(d23) * rs[2], bf_hi(d23) * rs[3]);
        float uu[4] = {bf_lo(u01), bf_hi(u01), bf_lo(u23), bf_hi(u23)};
        u32 s01 = pk_bf(uu[0] * ds[ch], uu[1] * ds[ch]), s23 = pk_bf(uu[2] * ds[ch], uu[3] * ds[ch]);
        *(u32x2*)(U + ch * UP + 512 + t * 8) = u32x2{pk_h(uu[0], uu[1]), pk_h(uu[2], uu[3])};
        *(u32x4*)(XU + ch * 8192 + t * 16) = u32x4{xv[ch][0], s01, xv[ch][1], s23};
    }
    lds_barrier();
    if (w < 4) {
        const int ch = w >> 1, mh = w & 1;
        f32x4 acc[8];
#pragma unroll
        for (int i = 0; i < 8; ++i) acc[i] = f32x4{0.f, 0.f, 0.f, 0.f};
        const u32 lb = (u32)(uintptr_t)(__attribute__((address_space(3))) char*)sm;
        const u32 fb = lb + (ch * 4 + 3 - (r & 3)) * RCP + 2 * (2028 - 32 * (4 * mh) - 256 * 7 - (r & ~3) + 8 * g) - 192;
        const u32 ub = lb + 8 * RCP + ch * UP + 2 * (256 - 32 * (4 * mh) - 16 + 16 * r + 8 * g) - 192;
        struct CF { u32x2 lo[8], hi[8]; u32x4 b[8]; };
        CF c0f, c1f;
#define C_RDF(F, MU, I) do { \
        asm volatile("ds_read_b64 %0, %1 offset:%2" : "=v"(F.lo[I]) : "v"(fb), "n"(192 - 64 * (MU) + 512 * (7 - (I)))); \
        asm volatile("ds_read_b64 %0, %1 offset:%2" : "=v"(F.hi[I]) : "v"(fb), "n"(200 - 64 * (MU) + 512 * (7 - (I)))); } while (0)
#define C_RDB(F, MU, I) asm volatile("ds_read_b128 %0, %1 offset:%2" : "=v"(F.b[I]) : "v"(ub), "n"(192 - 64 * (MU) + 512 * (I)))
#define C_WAIT(F) do { asm volatile("s_waitcnt lgkmcnt(0)" : "+v"(F.lo[0]), "+v"(F.lo[1]), "+v"(F.lo[2]), "+v"(F.lo[3]), "+v"(F.lo[4]), "+v"(F.lo[5]), \
        "+v"(F.lo[6]), "+v"(F.lo[7]), "+v"(F.hi[0]), "+v"(F.hi[1]), "+v"(F.hi[2]), "+v"(F.hi[3]), "+v"(F.hi[4]), "+v"(F.hi[5]), "+v"(F.hi[6]), "+v"(F.hi[7])); \
        asm volatile("" : "+v"(F.b[0]), "+v"(F.b[1]), "+v"(F.b[2]), "+v"(F.b[3]), "+v"(F.b[4]), "+v"(F.b[5]), "+v"(F.b[6]), "+v"(F.b[7])); } while (0)
#define C_FR(F, I) __builtin_bit_cast(f16x8, u32x4{F.lo[I][0], F.lo[I][1], F.hi[I][0], F.hi[I][1]})
#define C_STEP(CUR, NXT, MUN, PF) do { \
        _Pragma("unroll") for (int a = 0; a < 8; ++a) { \
            _Pragma("unroll") for (int q = 7; q >= 0; --q) { \
                if (q < 8 - a) { \
                    const int slot = a * 8 - a * (a - 1) / 2 + (7 - a - q); \
                    if (slot % 5 == 0) { \
                        __builtin_amdgcn_sched_barrier(0); \
                        if (PF) { C_RDF(NXT, MUN, slot / 5); C_RDB(NXT, MUN, slot / 5); } \
                    } \
                    acc[a + q] = mfma_h(C_FR(CUR, a), __builtin_bit_cast(f16x8, CUR.b[q]), acc[a + q]); \
                } } } \
        __builtin_amdgcn_sched_barrier(0); } while (0)
#pragma unroll
        for (int i = 0; i < 8; ++i) { C_RDF(c0f, 0, i); C_RDB(c0f, 0, i); }
        C_WAIT(c0f);
        __builtin_amdgcn_sched_barrier(0);
        C_STEP(c0f, c1f, 1, true);  C_WAIT(c1f);
        C_STEP(c1f, c0f, 2, true);  C_WAIT(c0f);
        C_STEP(c0f, c1f, 3, true);  C_WAIT(c1f);
        C_STEP(c1f, c0f, 0, false);
#undef C_RDF
#undef C_RDB
#undef C_WAIT
#undef C_FR
#undef C_STEP
#pragma unroll
        for (int i = 0; i < 8; ++i) *(f32x4*)(RED + ((ch * 2 + mh) * 8 + i) * 1024 + lane * 16) = acc[i];
    }
    lds_barrier();
    const int ch = w >> 2;
    u16* dst = xo + (size_t)(c0 + ch) * XP;
#pragma unroll
    for (int i = 0; i < 2; ++i) {
        const int tt = 2 * (w & 3) + i;
        f32x4 y = (*(const f32x4*)(RED + (ch * 16 + tt) * 1024 + lane * 16) + *(const f32x4*)(RED + (ch * 16 + 8 + tt) * 1024 + lane * 16)) * ksc;
        int l = 256 * tt + 16 * r + 4 * g;
        u32x4 xu = *(const u32x4*)(XU + ch * 8192 + l * 4);
        u32 y01 = pk_bf(y[0], y[1]), y23 = pk_bf(y[2], y[3]);
        u32 z01 = pk_bf(bf_lo(y01) + bf_lo(xu[1]), bf_hi(y01) + bf_hi(xu[1])), z23 = pk_bf(bf_lo(y23) + bf_lo(xu[3]), bf_hi(y23) + bf_hi(xu[3]));
        st8(dst + l, u32x2{pk_bf(bf_lo(xu[0]) + bf_lo(z01), bf_hi(xu[0]) + bf_hi(z01)),
                           pk_bf(bf_lo(xu[2]) + bf_lo(z23), bf_hi(xu[2]) + bf_hi(z23))});
    }
}

constexpr int QSLOT = 28672, QNS = 5;
DEVI u32 phi2(f32x2 a) {
    const u32 tb = pk_bf(a[0], a[1]);
    const f32x2 tq = {bf_lo(tb), bf_hi(tb)};
    const f32x2 mn = {__builtin_amdgcn_fmed3f(tq[0], -__builtin_inff(), 0.f), __builtin_amdgcn_fmed3f(tq[1], -__builtin_inff(), 0.f)};
    const f32x2 me = mn * 1.4426950408889634f;
    const f32x2 p = f32x2{__builtin_amdgcn_exp2f(me[0]), __builtin_amdgcn_exp2f(me[1])} + (tq - mn);
    const u32 pb = pk_bf(p[0], p[1]);
    const f32x2 p2 = f32x2{bf_lo(pb), bf_hi(pb)} + 1e-6f;
    return pk_bf(p2[0], p2[1]);
}
template <int N> DEVI void wait_vm() { asm volatile("s_waitcnt vmcnt(%0)" ::"n"(N) : "memory"); }

__global__ __launch_bounds__(512, 1) void qkv_kernel(
    const u16* __restrict__ xT, const u16* __restrict__ wqF, const float* __restrict__ Wsum,
    u16* __restrict__ pqF, float* __restrict__ kvacc) {
    __shared__ __attribute__((aligned(16))) char ring[QNS * QSLOT + 4096];
    float* lnm = (float*)(ring + QNS * QSLOT);
    float* lnr = lnm + 128;
    float* wst = lnr + 128;
    const int t = threadIdx.x, lane = t & 63, g = lane >> 4, j = lane & 15;
    const int w = __builtin_amdgcn_readfirstlane(t >> 6), rg = (w >> 1) & 1, hh = w & 1;
    const int xcd = blockIdx.x & 7, jb = blockIdx.x >> 3;
    const int rt = 4 * (xcd >> 1) + (jb & 3), hp = 8 * (xcd & 1) + (jb >> 2);
    f32x4 aq[4], ak[4], av[4];
    char* hand = ring;
    if (w >= 4) {
        const int lw = w - 4;
        const u16* xs[4];
#pragma unroll
        for (int i = 0; i < 4; ++i) {
            int row = 4 * (4 * lw + i) + g, c16 = j;
            int f = (row & 3) | (((row >> 3) & 1) << 2);
            xs[i] = xT + (size_t)row * XP + rt * 128 + ((c16 >> 1) ^ f) * 16 + (c16 & 1) * 8;
        }
        const u16* wsrc = wqF + (size_t)hp * (16 * 6 * 512) + (3 * lw) * 512 + lane * 8;
        auto issue = [&](int kc) {
            char* slot = ring + (kc % QNS) * QSLOT;
#pragma unroll
            for (int i = 0; i < 4; ++i)
                __builtin_amdgcn_global_load_lds((const u32*)(xs[i] + (size_t)kc * 64 * XP), (u32*)(slot + (4 * lw + i) * 1024), 16, 0, 0);
#pragma unroll
            for (int i = 0; i < 3; ++i)
                __builtin_amdgcn_global_load_lds((const u32*)(wsrc + kc * (12 * 512) + i * 512), (u32*)(slot + 16384 + (3 * lw + i) * 1024), 16, 0, 0);
        };
        issue(0); issue(1);
        wait_vm<7>();
        __builtin_amdgcn_s_barrier();
        issue(2);
#pragma unroll
        for (int kc = 1; kc < 8; ++kc) {
            if (kc + 2 < 8) issue(kc + 2);
            if (kc <= 5) wait_vm<14>(); else if (kc == 6) wait_vm<7>(); else wait_vm<0>();
            __builtin_amdgcn_s_barrier();
        }
    } else {
        if (w == 0) {
            const int h2 = lane >= 48 ? 0 : lane;
            if (lane < 48) {
                const float* src = Wsum + (h2 / 16) * DM + 2 * hp * 16 + (h2 & 15);
                constexpr int P = 3 * DM;
                wst[h2] = (src[0] + src[P]) + (src[2 * P] + src[3 * P]);
                wst[48 + h2] = (src[16] + src[P + 16]) + (src[2 * P + 16] + src[3 * P + 16]);
            }
        }
        f32x4 as[4];
#pragma unroll
        for (int i = 0; i < 4; ++i) { aq[i] = ak[i] = av[i] = as[i] = f32x4{0.f, 0.f, 0.f, 0.f}; }
        const u32 ones = 0x3f803f80u;
        const bf16x8 onesv = __builtin_bit_cast(bf16x8, u32x4{ones, ones, ones, ones});
        const int q4 = (lane >> 2) & 3, fl = q4 | ((g & 1) << 2);
        const u32 lbase = (u32)(uintptr_t)(__attribute__((address_space(3))) char*)ring;
        u32 xoff[4];
#pragma unroll
        for (int mt = 0; mt < 4; ++mt) xoff[mt] = lbase + (8 * g + q4) * 256 + (((4 * rg + mt) ^ fl) * 32) + (lane & 3) * 8;
        const u32 woff = lbase + 16384 + hh * 3 * 1024 + lane * 16;
        struct Frag { u32x2 lo[4], hi[4]; u32x4 wq, wk, wv; };
        auto rd = [&](Frag& f, int kc, int ks) {
            const u32 so = (kc % QNS) * QSLOT;
#pragma unroll
            for (int mt = 0; mt < 4; ++mt) {
                u32 a = xoff[mt] + so;
                if (ks == 0) {
                    asm volatile("ds_read_b64_tr_b16 %0, %1" : "=v"(f.lo[mt]) : "v"(a));
                    asm volatile("ds_read_b64_tr_b16 %0, %1 offset:1024" : "=v"(f.hi[mt]) : "v"(a));
                } else {
                    asm volatile("ds_read_b64_tr_b16 %0, %1 offset:8192" : "=v"(f.lo[mt]) : "v"(a));
                    asm volatile("ds_read_b64_tr_b16 %0, %1 offset:9216" : "=v"(f.hi[mt]) : "v"(a));
                }
            }
            u32 a = woff + so;
            if (ks == 0) {
                asm volatile("ds_read_b128 %0, %1" : "=v"(f.wq) : "v"(a));
                asm volatile("ds_read_b128 %0, %1 offset:1024" : "=v"(f.wk) : "v"(a));
                asm volatile("ds_read_b128 %0, %1 offset:2048" : "=v"(f.wv) : "v"(a));
            } else {
                asm volatile("ds_read_b128 %0, %1 offset:6144" : "=v"(f.wq) : "v"(a));
                asm volatile("ds_read_b128 %0, %1 offset:7168" : "=v"(f.wk) : "v"(a));
                asm volatile("ds_read_b128 %0, %1 offset:8192" : "=v"(f.wv) : "v"(a));
            }
        };
#define Q_WAIT(F, N) asm volatile("s_waitcnt lgkmcnt(" #N ")" : "+v"(F.lo[0]), "+v"(F.hi[0]), "+v"(F.lo[1]), "+v"(F.hi[1]), \
        "+v"(F.lo[2]), "+v"(F.hi[2]), "+v"(F.lo[3]), "+v"(F.hi[3]), "+v"(F.wq), "+v"(F.wk), "+v"(F.wv) :: "memory")
        auto mm = [&](const Frag& f) {
            bf16x8 wq = __builtin_bit_cast(bf16x8, f.wq), wk = __builtin_bit_cast(bf16x8, f.wk), wv = __builtin_bit_cast(bf16x8, f.wv);
            bf16x8 xa[4];
#pragma unroll
            for (int mt = 0; mt < 4; ++mt) xa[mt] = __builtin_bit_cast(bf16x8, u32x4{f.lo[mt][0], f.lo[mt][1], f.hi[mt][0], f.hi[mt][1]});
#pragma unroll
            for (int mt = 0; mt < 4; ++mt) {
                aq[mt] = mfma_bf(wq, xa[mt], aq[mt]);
                ak[mt] = mfma_bf(xa[mt], wk, ak[mt]);
                av[mt] = mfma_bf(xa[mt], wv, av[mt]);
            }
            if (hh) {
#pragma unroll
                for (int i = 0; i < 2; ++i) { as[i] = mfma_bf(xa[2 + i], xa[2 + i], as[i]); as[2 + i] = mfma_bf(xa[2 + i], onesv, as[2 + i]); }
            } else {
#pragma unroll
                for (int i = 0; i < 2; ++i) { as[i] = mfma_bf(xa[i], xa[i], as[i]); as[2 + i] = mfma_bf(xa[i], onesv, as[2 + i]); }
            }
        };
        Frag fa, fb;
        asm volatile("s_waitcnt vmcnt(0) lgkmcnt(0)" ::: "memory");
        __builtin_amdgcn_s_barrier();
        asm volatile("" ::: "memory");
        rd(fa, 0, 0);
#pragma unroll
        for (int kc = 0; kc < 8; ++kc) {
            rd(fb, kc, 1);
            Q_WAIT(fa, 11);
            mm(fa);
            Q_WAIT(fb, 0);
            if (kc < 7) {
                __builtin_amdgcn_s_barrier();
                asm volatile("" ::: "memory");
                rd(fa, kc + 1, 0);
            }
            mm(fb);
        }
#undef Q_WAIT
#pragma unroll
        for (int i = 0; i < 2; ++i) {
            *(f32x4*)(hand + ((w * 6 + 3 * i) * 64 + lane) * 16) = aq[2 + i];
            *(f32x4*)(hand + ((w * 6 + 3 * i + 1) * 64 + lane) * 16) = ak[2 + i];
            *(f32x4*)(hand + ((w * 6 + 3 * i + 2) * 64 + lane) * 16) = av[2 + i];
        }
#pragma unroll
        for (int i = 0; i < 2; ++i) {
            const int rgi = j & 3;
            const float sq = rgi == 0 ? as[i][0] : (rgi == 1 ? as[i][1] : (rgi == 2 ? as[i][2] : as[i][3]));
            const float meanj = (rgi == 0 ? as[2 + i][0] : (rgi == 1 ? as[2 + i][1] : (rgi == 2 ? as[2 + i][2] : as[2 + i][3]))) * (1.0f / DM);
            float mb, rs;
            ln_scalars(meanj, fmaxf(fmaf(-meanj, meanj, sq * (1.0f / DM)), 0.f), mb, rs);
            if (g == (j >> 2)) { lnm[rg * 64 + (2 * hh + i) * 16 + j] = mb; lnr[rg * 64 + (2 * hh + i) * 16 + j] = rs; }
        }
    }
    lds_barrier();
    if (w >= 4) {
#pragma unroll
        for (int i = 0; i < 2; ++i) {
            aq[i] = *(const f32x4*)(hand + (((w - 4) * 6 + 3 * i) * 64 + lane) * 16);
            ak[i] = *(const f32x4*)(hand + (((w - 4) * 6 + 3 * i + 1) * 64 + lane) * 16);
            av[i] = *(const f32x4*)(hand + (((w - 4) * 6 + 3 * i + 2) * 64 + lane) * 16);
        }
    }
    const int mt0 = (w >> 2) * 2;
    const f32x4 wsq = *(const f32x4*)(wst + hh * 48 + 4 * g);
    const float wsk = wst[hh * 48 + 16 + j], wsv = wst[hh * 48 + 32 + j];
    u32 pk8[4], vv8[4], pqo[4];
#pragma unroll
    for (int i = 0; i < 2; ++i) {
        const int r0 = rg * 64 + (mt0 + i) * 16;
        const float m1 = lnm[r0 + j], r1 = lnr[r0 + j];
        const u32 q01 = phi2((f32x2{aq[i][0], aq[i][1]} - f32x2{wsq[0], wsq[1]} * m1) * r1);
        const u32 q23 = phi2((f32x2{aq[i][2], aq[i][3]} - f32x2{wsq[2], wsq[3]} * m1) * r1);
        pqo[2 * i] = q01; pqo[2 * i + 1] = q23;
        const f32x4 mr = *(const f32x4*)(lnm + r0 + 4 * g), rr = *(const f32x4*)(lnr + r0 + 4 * g);
        const f32x2 m01 = {mr[0], mr[1]}, m23 = {mr[2], mr[3]}, r01 = {rr[0], rr[1]}, r23 = {rr[2], rr[3]};
        pk8[2 * i] = phi2((f32x2{ak[i][0], ak[i][1]} - m01 * wsk) * r01);
        pk8[2 * i + 1] = phi2((f32x2{ak[i][2], ak[i][3]} - m23 * wsk) * r23);
        const f32x2 v01 = (f32x2{av[i][0], av[i][1]} - m01 * wsv) * r01, v23 = (f32x2{av[i][2], av[i][3]} - m23 * wsv) * r23;
        vv8[2 * i] = pk_bf(v01[0], v01[1]); vv8[2 * i + 1] = pk_bf(v23[0], v23[1]);
    }
    st16(pqF + ((size_t)(((rt * 4 + rg * 2 + (mt0 >> 1)) * 16 + hp) * 2 + hh) * 64 + lane) * 8, u32x4{pqo[0], pqo[1], pqo[2], pqo[3]});
    const u32 ones = 0x3f803f80u;
    const bf16x8 pk = __builtin_bit_cast(bf16x8, u32x4{pk8[0], pk8[1], pk8[2], pk8[3]});
    const bf16x8 vv = __builtin_bit_cast(bf16x8, u32x4{vv8[0], vv8[1], vv8[2], vv8[3]});
    const f32x4 z4 = {0.f, 0.f, 0.f, 0.f};
    const f32x4 kvT = mfma_bf(vv, pk, z4);
    const f32x4 zz = mfma_bf(__builtin_bit_cast(bf16x8, u32x4{ones, ones, ones, ones}), pk, z4);
    float* red = (float*)(ring + QSLOT);
    float* rp = red + (hh * 4 + (w >> 1)) * KVH;
#pragma unroll
    for (int e = 0; e < 4; ++e) rp[(4 * g + e) * 16 + j] = kvT[e];
    if (g == 0) rp[256 + j] = zz[0];
    lds_barrier();
    for (int i = t; i < 2 * KVH; i += 512) {
        int h2 = i >= KVH, ii = i - h2 * KVH;
        const float* rq = red + h2 * 4 * KVH + ii;
        unsafeAtomicAdd(kvacc + (2 * hp + h2) * KVH + ii, (rq[0] + rq[KVH]) + (rq[2 * KVH] + rq[3 * KVH]));
    }
}

template <int FINAL>
__global__ __launch_bounds__(512, 1) void oproj_kernel(
    const u16* __restrict__ xT, const u16* __restrict__ pqF, const float* __restrict__ kvacc,
    const u16* __restrict__ woF, u16* __restrict__ xo, f32x2* __restrict__ stats) {
    __shared__ __attribute__((aligned(16))) char sm[32768 + 128 * 72 + 32 * 272 + 2048];
    char* OF = sm; char* XT = sm + 32768; char* OT = XT + 128 * 72; f32x2* ST = (f32x2*)(OT + 32 * 272);
    const int t = threadIdx.x, lane = t & 63, w = t >> 6, g = lane >> 4, j = lane & 15;
    const int xcd = blockIdx.x & 7, jb = blockIdx.x >> 3;
    const int rt = xcd * 8 + (jb >> 2), cq = jb & 3, l0 = rt * 32;
    u32x4 pq[2][2];
    {
        u32x4 pa[2][2];
#pragma unroll
        for (int i = 0; i < 2; ++i)
#pragma unroll
            for (int h2 = 0; h2 < 2; ++h2) pa[i][h2] = *(const u32x4*)(pqF + ((size_t)((rt * 16 + 2 * w + i) * 2 + h2) * 64 + lane) * 8);
#pragma unroll
        for (int lt = 0; lt < 2; ++lt)
#pragma unroll
            for (int i = 0; i < 2; ++i) pq[lt][i] = u32x4{pa[i][0][2 * lt], pa[i][0][2 * lt + 1], pa[i][1][2 * lt], pa[i][1][2 * lt + 1]};
    }
    f32x4 kf[4], zf[4];
#pragma unroll
    for (int i = 0; i < 4; ++i) {
        kf[i] = *(const f32x4*)(kvacc + (4 * w + i) * KVH + j * 16 + 4 * g);
        zf[i] = *(const f32x4*)(kvacc + (4 * w + i) * KVH + 256 + 4 * g);
    }
    u32x4 xin = *(const u32x4*)(xT + (size_t)(cq * 128 + (t >> 2)) * XP + l0 + (t & 3) * 8);
    bf16x8 wf[16];
#pragma unroll
    for (int ks = 0; ks < 16; ++ks) wf[ks] = *(const bf16x8*)(woF + ((size_t)((cq * 8 + w) * 16 + ks) * 64 + lane) * 8);
    {
        char* p = XT + (t >> 2) * 72 + (t & 3) * 16;
        *(u32x2*)p = u32x2{xin[0], xin[1]}; *(u32x2*)(p + 8) = u32x2{xin[2], xin[3]};
    }
#pragma unroll
    for (int i = 0; i < 2; ++i) {
        u32x4 of[2];
#pragma unroll
        for (int h2 = 0; h2 < 2; ++h2) {
            f32x4 k4 = kf[2 * i + h2], z4 = zf[2 * i + h2];
            s16x4 ka = __builtin_bit_cast(s16x4, u32x2{pk_bf(k4[0], k4[1]), pk_bf(k4[2], k4[3])});
            s16x4 za = __builtin_bit_cast(s16x4, u32x2{pk_bf(z4[0], z4[1]), pk_bf(z4[2], z4[3])});
#pragma unroll
            for (int lt = 0; lt < 2; ++lt) {
                s16x4 b = __builtin_bit_cast(s16x4, u32x2{pq[lt][i][2 * h2], pq[lt][i][2 * h2 + 1]});
                f32x4 zero = {0.f, 0.f, 0.f, 0.f};
                f32x4 num = mfma_bf16k(ka, b, zero), den = mfma_bf16k(za, b, zero);
                float rd = __builtin_amdgcn_rcpf(rbf(rbf(den[0]) + 1e-6f));
                const u32 n01 = pk_bf(num[0], num[1]), n23 = pk_bf(num[2], num[3]);
                const f32x2 o01 = f32x2{bf_lo(n01), bf_hi(n01)} * rd, o23 = f32x2{bf_lo(n23), bf_hi(n23)} * rd;
                of[lt][2 * h2] = pk_bf(o01[0], o01[1]);
                of[lt][2 * h2 + 1] = pk_bf(o23[0], o23[1]);
            }
        }
#pragma unroll
        for (int lt = 0; lt < 2; ++lt) *(u32x4*)(OF + (lt * 16 + 2 * w + i) * 1024 + lane * 16) = of[lt];
    }
    lds_barrier();
    u16 xr[2][4];
#pragma unroll
    for (int lt = 0; lt < 2; ++lt)
#pragma unroll
        for (int e = 0; e < 4; ++e) xr[lt][e] = *((const u16*)(XT + (16 * w + 4 * g + e) * 72) + 16 * lt + j);
    __builtin_amdgcn_sched_barrier(0);
    f32x4 acc[2] = {{0.f, 0.f, 0.f, 0.f}, {0.f, 0.f, 0.f, 0.f}};
#pragma unroll
    for (int ks = 0; ks < 16; ++ks)
#pragma unroll
        for (int lt = 0; lt < 2; ++lt)
            acc[lt] = mfma_bf(wf[ks], *(const bf16x8*)(OF + (lt * 16 + ks) * 1024 + lane * 16), acc[lt]);
#pragma unroll
    for (int lt = 0; lt < 2; ++lt) {
        float r[4];
#pragma unroll
        for (int e = 0; e < 4; ++e) {
            u16* px = (u16*)(XT + (16 * w + 4 * g + e) * 72) + 16 * lt + j;
            r[e] = rbf(bf2f(xr[lt][e]) + rbf(acc[lt][e]));
            if (!FINAL) *px = (u16)(__float_as_uint(r[e]) >> 16);
        }
        if (FINAL) {
            *(u32x2*)(OT + (16 * lt + j) * 272 + (16 * w + 4 * g) * 2) = u32x2{pk_bf(r[0], r[1]), pk_bf(r[2], r[3])};
        } else {
            float s = (r[0] + r[1]) + (r[2] + r[3]);
            s = quad_rows_sum(s);
            float mean = s * (1.0f / 16.0f), q = 0.f;
#pragma unroll
            for (int e = 0; e < 4; ++e) { float d = r[e] - mean; q = fmaf(d, d, q); }
            q = quad_rows_sum(q);
            if (g == 0) ST[w * 32 + 16 * lt + j] = f32x2{mean, q};
        }
    }
    lds_barrier();
    if (FINAL) {
        u32x4 o = *(const u32x4*)(OT + (t >> 4) * 272 + (t & 15) * 16);
        st16(xo + (size_t)(l0 + (t >> 4)) * DM + cq * 128 + (t & 15) * 8, o);
    } else {
        const char* p = XT + (t >> 2) * 72 + (t & 3) * 16;
        u32x2 a = *(const u32x2*)p, c = *(const u32x2*)(p + 8);
        st16(xo + (size_t)(cq * 128 + (t >> 2)) * XP + l0 + (t & 3) * 8, u32x4{a[0], a[1], c[0], c[1]});
        if (t < 32) {
            float mean = 0.f, m2 = 0.f;
#pragma unroll
            for (int i = 0; i < 8; ++i) { f32x2 s = ST[i * 32 + t]; mean += s[0]; m2 += s[1]; }
            mean *= 0.125f;
#pragma unroll
            for (int i = 0; i < 8; ++i) { float d = ST[i * 32 + t][0] - mean; m2 = fmaf(16.0f * d, d, m2); }
            stats[cq * SL + l0 + t] = f32x2{mean, m2};
        }
    }
}

extern "C" void kernel_launch(void* const* d_in, const int* in_sizes, int n_in,
                              void* d_out, int out_size, void* d_ws, size_t ws_size,
                              hipStream_t stream) {
    const u16* u = (const u16*)d_in[0];
    const u16* lar = (const u16*)d_in[1];
    const u16* aim = (const u16*)d_in[2];
    const u16* cre = (const u16*)d_in[3];
    const u16* cim = (const u16*)d_in[4];
    const u16* ldt = (const u16*)d_in[5];
    const u16* dsk = (const u16*)d_in[6];
    const u16* wqkv = (const u16*)d_in[8];
    const u16* wo = (const u16*)d_in[9];
    char* ws = (char*)d_ws;
    const size_t MB = 1 << 20;
    u16* xT0 = (u16*)(ws + 18 * MB);
    u16* xT1 = (u16*)(ws + 21 * MB);
    u16* xT2 = (u16*)(ws + 24 * MB);
    u16* xT3 = (u16*)(ws + 27 * MB);
    u16* pqF = (u16*)(ws + 8 * MB);
    u16* Kr = (u16*)(ws + 10 * MB);
    u16* wqF = (u16*)(ws + 13 * MB);
    u16* woF = (u16*)(ws + 15 * MB);
    f32x2* st0 = (f32x2*)(ws + 16 * MB);
    f32x2* st2 = (f32x2*)(ws + 16 * MB + 65536);
    float* Kscale = (float*)(ws + 16 * MB + 196608);
    float* Wsum = (float*)(ws + 16 * MB + 200704);
    float* kvacc = (float*)(ws + 16 * MB + 262144);
    prep_kernel<<<P_TR + P_KG, 256, 0, stream>>>(u, lar, aim, cre, cim, ldt, wqkv, wo, xT0, st0, Kr, Kscale,
                                                               wqF, Wsum, woF, kvacc);
    conv_kernel<1><<<256, 512, 0, stream>>>(xT0, st0, Kr, Kscale, dsk, xT1);
    qkv_kernel<<<256, 512, 0, stream>>>(xT1, wqF, Wsum, pqF, kvacc);
    oproj_kernel<0><<<256, 512, 0, stream>>>(xT1, pqF, kvacc, woF, xT2, st2);
    conv_kernel<4><<<256, 512, 0, stream>>>(xT2, st2, Kr, Kscale, dsk, xT3);
    qkv_kernel<<<256, 512, 0, stream>>>(xT3, wqF, Wsum, pqF, kvacc + 32 * KVH);
    oproj_kernel<1><<<256, 512, 0, stream>>>(xT3, pqF, kvacc + 32 * KVH, woF, (u16*)d_out, st2);
}
```
